```python
import torch

def _softmax(x, axis=-1):
    return torch.softmax(x, dim=axis)
def _gelu(x):
    return torch.nn.functional.gelu(x, approximate="tanh")
def _silu(x):
    return torch.nn.functional.silu(x)
def _sigmoid(x):
    return torch.sigmoid(x)
def _layernorm(x, axis=-1, eps=1e-5):
    m = x.mean(dim=axis, keepdim=True)
    v = x.var(dim=axis, keepdim=True, unbiased=False)
    return (x - m) / torch.sqrt(v + eps)
def _scatter_add(tgt, idx, src, axis=0):
    return tgt.clone().index_add_(axis, idx, src)
def _segment_sum(src, seg, n):
    out = torch.zeros((n,) + src.shape[1:], dtype=src.dtype)
    return out.index_add_(0, seg, src)
def _pad(x, pw):
    flat = tuple(v for pair in reversed(pw) for v in pair)
    return torch.nn.functional.pad(x, flat)

kernel_name = 'concat_heads__spectral_fnet__mha_block__prompt'

def setup_inputs():

    _w = (lambda a: (lambda t: t.to(torch.bfloat16) if t.is_floating_point() else t)(torch.as_tensor(a)))
    def _A_setup():
        B, S, D = 1, 2048, 1024
        ns = torch.arange(S)[:, None] * torch.arange(S)[None, :]
        nd = torch.arange(D)[:, None] * torch.arange(D)[None, :]
        Ws_re = torch.cos(-2.0 * torch.pi * ns / S).to(float)
        Ws_im = torch.sin(-2.0 * torch.pi * ns / S).to(float)
        Wd_re = torch.cos(-2.0 * torch.pi * nd / D).to(float)
        Wd_im = torch.sin(-2.0 * torch.pi * nd / D).to(float)
        return {
            "x": _w((lambda *s: torch.randn(*s, dtype=torch.bfloat16) * 0.05)(B, S, D)),
            "dft_seq_re": _w(Ws_re),
            "dft_seq_im": _w(Ws_im),
            "dft_hid_re": _w(Wd_re),
            "dft_hid_im": _w(Wd_im),
        }
    _A_in = _A_setup()

    def _B_setup():
        B, S, D, H = 1, 2048, 1024, 32
        return {
            "x": _w((lambda *s: torch.randn(*s, dtype=torch.bfloat16) * 0.2)(B, S, D)),
            "wqkv": _w((lambda *s: torch.randn(*s, dtype=torch.bfloat16) * 0.8)(D, 3 * D)),
            "wo": _w((lambda *s: torch.randn(*s, dtype=torch.bfloat16) * 0.2)(D, D)),
        }
    _B_in = _B_setup()

    return {
        "A_x": _A_in["x"],
        "A_dft_seq_re": _A_in["dft_seq_re"],
        "A_dft_seq_im": _A_in["dft_seq_im"],
        "A_dft_hid_re": _A_in["dft_hid_re"],
        "A_dft_hid_im": _A_in["dft_hid_im"],
        "B_x": _B_in["x"],
        "B_wqkv": _B_in["wqkv"],
        "B_wo": _B_in["wo"],
    }

def reference(A_x, A_dft_seq_re, A_dft_seq_im, A_dft_hid_re, A_dft_hid_im, B_x, B_wqkv, B_wo):
    def _A_ref(x, dft_seq_re, dft_seq_im, dft_hid_re, dft_hid_im):
        a = torch.einsum("bsd,ed->bse", x, dft_hid_re)
        b = torch.einsum("bsd,ed->bse", x, dft_hid_im)
        y = torch.einsum("ts,bse->bte", dft_seq_re, a) - torch.einsum("ts,bse->bte", dft_seq_im, b)
        return y

    def _B_ref(x, wqkv, wo):
        B, S, D = x.shape
        H = 32
        Dh = D // H
        qkv = (lambda x, *d: x.permute(*d))((x @ wqkv).reshape(B, S, 3, H, Dh), 2, 0, 3, 1, 4)
        q, k, v = qkv[0], qkv[1], qkv[2]
        scores = torch.einsum("bhqd,bhkd->bhqk", q, k) * (Dh ** -0.5)
        p = _softmax(scores, axis=-1)
        o = torch.einsum("bhqk,bhkd->bhqd", p, v)
        return (lambda x, *d: x.permute(*d))(o, 0, 2, 1, 3).reshape(B, S, D) @ wo

    a = _A_ref(A_x, A_dft_seq_re, A_dft_seq_im, A_dft_hid_re, A_dft_hid_im)
    b = _B_ref(B_x, B_wqkv, B_wo)
    return torch.concatenate([a, b], axis=-1)
```

```cpp
#include <hip/hip_runtime.h>
#include <hip/hip_bf16.h>

typedef short bf16x8 __attribute__((ext_vector_type(8)));
typedef short s16x4 __attribute__((ext_vector_type(4)));
typedef float f32x16 __attribute__((ext_vector_type(16)));
typedef float f32x4 __attribute__((ext_vector_type(4)));
typedef unsigned u32x4 __attribute__((ext_vector_type(4)));
#define LAS __attribute__((address_space(3)))
typedef LAS const char* lds_cptr;

#define MFMA32(a, b, c) __builtin_amdgcn_mfma_f32_32x32x16_bf16(a, b, c, 0, 0, 0)
#define MFMA16(a, b, c) __builtin_amdgcn_mfma_f32_16x16x32_bf16(a, b, c, 0, 0, 0)
#define SBAR() __builtin_amdgcn_sched_barrier(0)
#define PIN(x) asm volatile("" : "+v"(x))

constexpr int S_ = 2048, D_ = 1024;
constexpr int LDS_BYTES = 81920;
constexpr size_t WS_AB = 0;
constexpr size_t WS_Q = WS_AB + (8u << 20);
constexpr size_t WS_KV = WS_Q + (4u << 20);
constexpr size_t WS_O = WS_KV + (8u << 20);
constexpr size_t WS_PIM = WS_O + (4u << 20);

typedef float f32x2 __attribute__((ext_vector_type(2)));
typedef __bf16 bf16x2_t __attribute__((ext_vector_type(2)));
__device__ __forceinline__ unsigned cvtpk(float lo, float hi) { return __builtin_bit_cast(unsigned, __builtin_convertvector((f32x2){lo, hi}, bf16x2_t)); }
__device__ __forceinline__ u32x4 pack8(const f32x16& a, int o, float sc) {
    return (u32x4){cvtpk(a[o] * sc, a[o + 1] * sc), cvtpk(a[o + 2] * sc, a[o + 3] * sc), cvtpk(a[o + 4] * sc, a[o + 5] * sc), cvtpk(a[o + 6] * sc, a[o + 7] * sc)}; }
__device__ __forceinline__ u32x4 pack8(const f32x16& a, int o) {
    return (u32x4){cvtpk(a[o], a[o + 1]), cvtpk(a[o + 2], a[o + 3]), cvtpk(a[o + 4], a[o + 5]), cvtpk(a[o + 6], a[o + 7])}; }
__device__ __forceinline__ bf16x8 ldsr(lds_cptr p) { return *(const LAS bf16x8*)p; }
typedef short v4i16_t __attribute__((ext_vector_type(4)));
__device__ __forceinline__ s16x4 ldstr(lds_cptr p) { return __builtin_bit_cast(s16x4, __builtin_amdgcn_ds_read_tr16_b64_v4i16((LAS v4i16_t*)p)); }
__device__ __forceinline__ bf16x8 cat(s16x4 a, s16x4 b) { return (bf16x8){a[0], a[1], a[2], a[3], b[0], b[1], b[2], b[3]}; }

__device__ __forceinline__ void dma4(const char* sbase, unsigned lds, unsigned v0, unsigned v1, unsigned v2, unsigned v3) {
    asm volatile("s_mov_b32 m0, %1\n\ts_nop 0\n\t"
                 "global_load_lds_dwordx4 %2, %0\n\t"
                 "global_load_lds_dwordx4 %3, %0 offset:1024\n\t"
                 "global_load_lds_dwordx4 %4, %0 offset:2048\n\t"
                 "global_load_lds_dwordx4 %5, %0 offset:3072"
                 :: "s"(sbase), "s"(lds), "v"(v0), "v"(v1), "v"(v2), "v"(v3) : "memory"); }
__device__ __forceinline__ void dma2(const char* sbase, unsigned lds, unsigned v0, unsigned v1) {
    asm volatile("s_mov_b32 m0, %1\n\ts_nop 0\n\t"
                 "global_load_lds_dwordx4 %2, %0\n\t"
                 "global_load_lds_dwordx4 %3, %0 offset:1024"
                 :: "s"(sbase), "s"(lds), "v"(v0), "v"(v1) : "memory"); }
__device__ __forceinline__ void gld4(const char* sbase, unsigned voff, bf16x8& a, bf16x8& b, bf16x8& c, bf16x8& d) {
    asm volatile("global_load_dwordx4 %0, %5, %4\n\t"
                 "global_load_dwordx4 %1, %5, %4 offset:1024\n\t"
                 "global_load_dwordx4 %2, %5, %4 offset:2048\n\t"
                 "global_load_dwordx4 %3, %5, %4 offset:3072"
                 : "=&v"(a), "=&v"(b), "=&v"(c), "=&v"(d) : "s"(sbase), "v"(voff) : "memory"); }
#define WAIT_BAR(N) asm volatile("s_waitcnt vmcnt(" #N ") lgkmcnt(0)\n\ts_barrier" ::: "memory")
#define WAIT_BAR4(N, R) asm volatile("s_waitcnt vmcnt(" #N ") lgkmcnt(0)\n\ts_barrier" : "+v"(R[0]), "+v"(R[1]), "+v"(R[2]), "+v"(R[3]) :: "memory")

__device__ __forceinline__ int swap23(int r) { return (r & 19) | ((r & 4) << 1) | ((r & 8) >> 1); }
__device__ __forceinline__ int emap(int r) { return ((r & 4) << 2) | ((r & 16) >> 1) | ((r & 8) >> 1) | (r & 3); }
__device__ __forceinline__ unsigned krow_off(int row0, int j, int lane, int ld) {
    const int r = row0 + 8 * j + (lane >> 3);
    return (unsigned)(r * ld + (((lane & 7) ^ ((r >> 1) & 7)) << 4) - 1024 * j); }

__device__ __forceinline__ void role_g1(int b, const char* xA, const char* wre, const char* wim, char* ws, char* lds) {
    const int lane = threadIdx.x & 63, wid = __builtin_amdgcn_readfirstlane(threadIdx.x >> 6);
    const int x = b & 7, i = b >> 3;
    const int sm = (x >> 2) * 8 + (i & 7), en = (x & 3) * 4 + (i >> 3);
    const int p = en >> 3, e0 = (en & 7) * 128, s0 = sm * 128;
    const char* ga = xA + (size_t)s0 * 2048;
    const char* gb = (p ? wim : wre) + (size_t)e0 * 2048;
    unsigned vo[4];
#pragma unroll
    for (int j = 0; j < 4; ++j) vo[j] = krow_off(wid * 32, j, lane, 2048);
    const unsigned lds0 = (unsigned)(uintptr_t)lds, ldw = lds0 + wid * 4096;
    const int wm = wid >> 1, wn = wid & 1, r = lane & 31, h = lane >> 5;
    const int ra = wm * 64 + swap23(r), rb = wn * 64 + emap(r);
    const int oa = ra * 128 + ((h ^ ((ra >> 1) & 7)) << 4), ob = 16384 + rb * 128 + ((h ^ ((rb >> 1) & 7)) << 4);
    f32x16 acc[2][2] = {};
    dma4(ga, ldw, vo[0], vo[1], vo[2], vo[3]); dma4(gb, ldw + 16384, vo[0], vo[1], vo[2], vo[3]);
#pragma unroll 2
    for (int kt = 0; kt < 16; ++kt) {
        WAIT_BAR(0);
        const int cur = (kt & 1) * 32768;
        if (kt + 1 < 16) { const unsigned d = ldw + (cur ^ 32768);
            dma4(ga + (kt + 1) * 128, d, vo[0], vo[1], vo[2], vo[3]); dma4(gb + (kt + 1) * 128, d + 16384, vo[0], vo[1], vo[2], vo[3]); }
        const lds_cptr base = (lds_cptr)lds + cur;
#pragma unroll
        for (int kk = 0; kk < 4; ++kk) {
            const bf16x8 a0 = ldsr(base + (oa ^ (kk * 32))), a1 = ldsr(base + (oa ^ (kk * 32)) + 4096);
            const bf16x8 b0 = ldsr(base + (ob ^ (kk * 32))), b1 = ldsr(base + (ob ^ (kk * 32)) + 4096);
            acc[0][0] = MFMA32(a0, b0, acc[0][0]); acc[0][1] = MFMA32(a0, b1, acc[0][1]);
            acc[1][0] = MFMA32(a1, b0, acc[1][0]); acc[1][1] = MFMA32(a1, b1, acc[1][1]);
        }
    }
    char* ab = ws + WS_AB + lane * 16;
#pragma unroll
    for (int um = 0; um < 2; ++um)
#pragma unroll
        for (int un = 0; un < 2; ++un) {
            const int eb = (e0 >> 5) + wn * 2 + un, sb = (s0 >> 4) + wm * 4 + um * 2;
            char* dst = ab + ((size_t)((p * 32 + eb) * 128 + sb) << 10);
            *(u32x4*)dst = pack8(acc[um][un], 0); *(u32x4*)(dst + 1024) = pack8(acc[um][un], 8);
        }
}

__device__ __forceinline__ void role_g2(int b, const char* xB, const char* wqkv, char* ws, char* lds) {
    const int lane = threadIdx.x & 63, wid = __builtin_amdgcn_readfirstlane(threadIdx.x >> 6);
    const int x = b & 7, i = b >> 3;
    const int tm = (x >> 2) * 8 + (i & 7), hp = (x & 3) * 4 + (i >> 3);
    const char* ga = xB + (size_t)tm * 128 * 2048;
    const char* gw = wqkv + hp * 128;
    unsigned vo[4], vw[2];
#pragma unroll
    for (int j = 0; j < 4; ++j) vo[j] = krow_off(wid * 32, j, lane, 2048);
#pragma unroll
    for (int j = 0; j < 2; ++j) { const int k = wid * 16 + 8 * j + (lane >> 3); vw[j] = (unsigned)(k * 6144 + (((lane & 7) ^ (((k >> 1) & 1) << 2)) << 4) - 1024 * j); }
    const unsigned lds0 = (unsigned)(uintptr_t)lds;
    const int wm = wid >> 1, wn = wid & 1, r = lane & 31, h = lane >> 5;
    const int ra = wm * 64 + r;
    const int oa = ra * 128 + ((h ^ ((ra >> 1) & 7)) << 4);
    const int q4 = (lane >> 2) & 3;
    const int ow = 16384 + (8 * h + q4) * 128 + ((wn ^ (q4 >> 1)) << 6) + ((lane >> 4) & 1) * 32 + (lane & 3) * 8;
    f32x16 acc[2][3] = {};
#define G2_STAGE(kt, buf) do { const unsigned d_ = lds0 + (buf); const char* w_ = gw + (size_t)(kt) * 64 * 6144; \
        dma4(ga + (kt) * 128, d_ + wid * 4096, vo[0], vo[1], vo[2], vo[3]); \
        dma2(w_, d_ + 16384 + wid * 2048, vw[0], vw[1]); dma2(w_ + 2048, d_ + 24576 + wid * 2048, vw[0], vw[1]); dma2(w_ + 4096, d_ + 32768 + wid * 2048, vw[0], vw[1]); } while (0)
    G2_STAGE(0, 0);
#pragma unroll 2
    for (int kt = 0; kt < 16; ++kt) {
        WAIT_BAR(0);
        const int cur = (kt & 1) * 40960;
        if (kt + 1 < 16) G2_STAGE(kt + 1, 40960 - cur);
        const lds_cptr base = (lds_cptr)lds + cur;
#pragma unroll
        for (int kk = 0; kk < 4; ++kk) {
            const bf16x8 a0 = ldsr(base + (oa ^ (kk * 32))), a1 = ldsr(base + (oa ^ (kk * 32)) + 4096);
            const lds_cptr wp = base + ow + kk * 2048;
            const bf16x8 wq = cat(ldstr(wp), ldstr(wp + 512)), wk = cat(ldstr(wp + 8192), ldstr(wp + 8192 + 512)), wv = cat(ldstr(wp + 16384), ldstr(wp + 16384 + 512));
            acc[0][0] = MFMA32(wq, a0, acc[0][0]); acc[1][0] = MFMA32(wq, a1, acc[1][0]);
            acc[0][1] = MFMA32(wk, a0, acc[0][1]); acc[1][1] = MFMA32(wk, a1, acc[1][1]);
            acc[0][2] = MFMA32(a0, wv, acc[0][2]); acc[1][2] = MFMA32(a1, wv, acc[1][2]);
        }
    }
#undef G2_STAGE
    const int hd = hp * 2 + wn;
    constexpr float QS = 0.17677669529663687f * 1.4426950408889634f;
#pragma unroll
    for (int u = 0; u < 2; ++u) {
        const int tt = tm * 4 + wm * 2 + u;
        char* q = ws + WS_Q + ((size_t)(hd * 64 + tt) << 11) + lane * 16;
        char* kv = ws + WS_KV + ((size_t)(hd * 64 + tt) << 12) + lane * 16;
        *(u32x4*)q = pack8(acc[u][0], 0, QS); *(u32x4*)(q + 1024) = pack8(acc[u][0], 8, QS);
        *(u32x4*)kv = pack8(acc[u][1], 0); *(u32x4*)(kv + 1024) = pack8(acc[u][1], 8);
        *(u32x4*)(kv + 2048) = pack8(acc[u][2], 0); *(u32x4*)(kv + 3072) = pack8(acc[u][2], 8);
    }
}

__device__ __forceinline__ void role_g3(int b, const char* wsre, const char* wsim, char* ws, char* out, char* lds) {
    const int lane = threadIdx.x & 63, wid = __builtin_amdgcn_readfirstlane(threadIdx.x >> 6);
    const int x = b & 7, i = b >> 3;
    const int p = x >> 2, tb = ((x >> 1) & 1) * 8 + (i & 7), eb = ((x & 1) * 4 + (i >> 3)) * 4 + wid;
    const char* gt = (p ? wsim : wsre) + (size_t)tb * 128 * 4096;
    const char* gf = ws + WS_AB + ((size_t)(p * 32 + eb) << 17);
    unsigned vo[4];
#pragma unroll
    for (int j = 0; j < 4; ++j) vo[j] = krow_off(wid * 32, j, lane, 4096);
    const unsigned vf = lane * 16;
    const unsigned ldw = (unsigned)(uintptr_t)lds + wid * 4096;
    const int r = lane & 31, h = lane >> 5;
    const int ob = r * 128 + ((h ^ ((r >> 1) & 7)) << 4);
    f32x16 acc[4] = {};
    bf16x8 f0[4], f1[4], f2[4], f3[4];
#define G3_ISSUE(k, slot, F) do { dma4(gt + (k) * 128, ldw + (slot) * 16384, vo[0], vo[1], vo[2], vo[3]); gld4(gf + (k) * 4096, vf, F[0], F[1], F[2], F[3]); } while (0)
#define G3_COMP(slot, F) do { const lds_cptr base_ = (lds_cptr)lds + (slot) * 16384; \
        _Pragma("unroll") for (int kk = 0; kk < 4; ++kk) { const lds_cptr q_ = base_ + (ob ^ (kk * 32)); \
            const bf16x8 t0 = ldsr(q_), t1 = ldsr(q_ + 4096), t2 = ldsr(q_ + 8192), t3 = ldsr(q_ + 12288); \
            acc[0] = MFMA32(F[kk], t0, acc[0]); acc[1] = MFMA32(F[kk], t1, acc[1]); acc[2] = MFMA32(F[kk], t2, acc[2]); acc[3] = MFMA32(F[kk], t3, acc[3]); } } while (0)
    G3_ISSUE(0, 0, f0); G3_ISSUE(1, 1, f1); G3_ISSUE(2, 2, f2);
    for (int k = 0; k < 28; k += 4) {
        WAIT_BAR4(16, f0); G3_ISSUE(k + 3, 3, f3); G3_COMP(0, f0);
        WAIT_BAR4(16, f1); G3_ISSUE(k + 4, 0, f0); G3_COMP(1, f1);
        WAIT_BAR4(16, f2); G3_ISSUE(k + 5, 1, f1); G3_COMP(2, f2);
        WAIT_BAR4(16, f3); G3_ISSUE(k + 6, 2, f2); G3_COMP(3, f3);
    }
    WAIT_BAR4(16, f0); G3_ISSUE(31, 3, f3); G3_COMP(0, f0);
    WAIT_BAR4(16, f1); G3_COMP(1, f1);
    WAIT_BAR4(8, f2); G3_COMP(2, f2);
    WAIT_BAR4(0, f3); G3_COMP(3, f3);
#undef G3_ISSUE
#undef G3_COMP
    char* dst = p ? ws + WS_PIM : out; const int pitch = p ? 2048 : 4096;
    dst += (size_t)(tb * 128 + r) * pitch + eb * 64 + h * 32;
#pragma unroll
    for (int u = 0; u < 4; ++u) { char* d = dst + (size_t)u * 32 * pitch; *(u32x4*)d = pack8(acc[u], 0); *(u32x4*)(d + 16) = pack8(acc[u], 8); }
}

__device__ __forceinline__ float xhalf_max(float m) { auto rr = __builtin_amdgcn_permlane32_swap(__float_as_uint(m), __float_as_uint(m), false, false);
    return __builtin_fmaxf(__uint_as_float(rr[0]), __uint_as_float(rr[1])); }
__device__ __forceinline__ float xhalf_sum(float m) { auto rr = __builtin_amdgcn_permlane32_swap(__float_as_uint(m), __float_as_uint(m), false, false);
    return __uint_as_float(rr[0]) + __uint_as_float(rr[1]); }
__device__ __forceinline__ float rowmax16(const f32x16& s) {
    float a = __builtin_fmaxf(s[0], s[1]);
#pragma unroll
    for (int r = 2; r < 16; r += 2) a = __builtin_fmaxf(__builtin_fmaxf(a, s[r]), s[r + 1]);
    return xhalf_max(a); }
__device__ __forceinline__ unsigned rescale_pk(unsigned w, int d) {
    return cvtpk(__builtin_ldexpf(__uint_as_float(w << 16), d), __builtin_ldexpf(__uint_as_float(w & 0xffff0000u), d)); }

__device__ __forceinline__ void role_att(int b, char* ws, char* lds) {
    const int lane = threadIdx.x & 63, wid = __builtin_amdgcn_readfirstlane(threadIdx.x >> 6);
    const int x = b & 7, i = b >> 3;
    const int hd = x * 4 + (i >> 3), tt0 = (i & 7) * 8 + wid * 2;
    const char* gkv = ws + WS_KV + ((size_t)hd << 18);
    const unsigned vl = lane * 16;
    const unsigned ldw = (unsigned)(uintptr_t)lds + wid * 4096;
#define AT_DMA(st) dma4(gkv + (st) * 16384 + wid * 4096, ldw + ((st) & 3) * 16384, vl, vl, vl, vl)
    AT_DMA(0); AT_DMA(1);
    const char* gq = ws + WS_Q + ((size_t)(hd * 64 + tt0) << 11) + vl;
    const bf16x8 q00 = *(const bf16x8*)gq, q01 = *(const bf16x8*)(gq + 1024), q10 = *(const bf16x8*)(gq + 2048), q11 = *(const bf16x8*)(gq + 3072);
    bf16x8 sel0, sel1;
    { const bool on = ((lane >> 2) & 1) == ((lane >> 4) & 1); const short v0 = (on && (lane & 3) == 0) ? (short)0x3F80 : (short)0, v1 = (on && (lane & 3) == 1) ? (short)0x3F80 : (short)0;
      sel0 = (bf16x8){v0, v0, v0, v0, v0, v0, v0, v0}; sel1 = (bf16x8){v1, v1, v1, v1, v1, v1, v1, v1}; PIN(sel0); PIN(sel1); }
    const lds_cptr lp = (lds_cptr)lds + vl;
    f32x16 o0 = {}, o1 = {}, nm0, nm1, sA, sB; f32x4 L = {};
    u32x4 pA0, pA1, pB0 = {}, pB1 = {};
    bf16x8 k0, k1, kn0, kn1, v0, v1, vp0 = {}, vp1 = {};
    WAIT_BAR(0);
    k0 = ldsr(lp); k1 = ldsr(lp + 1024);
    {
        f32x16 z = {};
        f32x16 s = MFMA32(k1, q01, MFMA32(k0, q00, z)); const float m0 = rowmax16(s) + 30.f;
        s = MFMA32(k1, q11, MFMA32(k0, q10, z)); const float m1 = rowmax16(s) + 30.f;
#pragma unroll
        for (int r = 0; r < 16; ++r) { nm0[r] = -m0; nm1[r] = -m1; }
        PIN(nm0); PIN(nm1);
    }
    sA = MFMA32(k1, q01, MFMA32(k0, q00, nm0));
#define EX(x) __builtin_amdgcn_exp2f(x)
#define BF(x) __builtin_bit_cast(bf16x8, x)
#define BODY(SC, SN, PC0, PC1, PP0, PP1, KA, KB, QA, QB, NM, VA, VB, OP, SEL, LD0, LD1) do { \
        SN = MFMA32(KA, QA, NM); SC[0] = EX(SC[0]); SC[1] = EX(SC[1]); SC[2] = EX(SC[2]); PIN(SC); SBAR(); \
        OP = MFMA32(VA, BF(PP0), OP); SC[3] = EX(SC[3]); SC[4] = EX(SC[4]); PC0[0] = cvtpk(SC[0], SC[1]); SC[5] = EX(SC[5]); PIN(SC); PIN(PC0); SBAR(); \
        SN = MFMA32(KB, QB, SN); LD0; SC[6] = EX(SC[6]); PC0[1] = cvtpk(SC[2], SC[3]); SC[7] = EX(SC[7]); PC0[2] = cvtpk(SC[4], SC[5]); SC[8] = EX(SC[8]); PIN(SC); PIN(PC0); SBAR(); \
        L = MFMA16(SEL, BF(PP0), L); SC[9] = EX(SC[9]); PC0[3] = cvtpk(SC[6], SC[7]); SC[10] = EX(SC[10]); PIN(SC); PIN(PC0); SBAR(); \
        OP = MFMA32(VB, BF(PP1), OP); LD1; SC[11] = EX(SC[11]); PC1[0] = cvtpk(SC[8], SC[9]); SC[12] = EX(SC[12]); SC[13] = EX(SC[13]); PC1[1] = cvtpk(SC[10], SC[11]); PIN(SC); PIN(PC1); SBAR(); \
        L = MFMA16(SEL, BF(PP1), L); SC[14] = EX(SC[14]); SC[15] = EX(SC[15]); PC1[2] = cvtpk(SC[12], SC[13]); PC1[3] = cvtpk(SC[14], SC[15]); PIN(PC1); SBAR(); \
    } while (0)
#define KTILE(KO, KN) do { \
        BODY(sA, sB, pA0, pA1, pB0, pB1, k0, k1, q10, q11, nm1, vp0, vp1, o1, sel1, (v0 = ldsr(lp + (KO) + 2048), v1 = ldsr(lp + (KO) + 3072)), (kn0 = ldsr(lp + (KN)), kn1 = ldsr(lp + (KN) + 1024))); \
        BODY(sB, sA, pB0, pB1, pA0, pA1, kn0, kn1, q00, q01, nm0, v0, v1, o0, sel0, (void)0, (void)0); \
        k0 = kn0; k1 = kn1; vp0 = v0; vp1 = v1; } while (0)
    for (int st = 0; st < 16; ++st) {
        if (st) WAIT_BAR(0);
        if (st + 2 < 16) AT_DMA(st + 2);
        if (__builtin_expect(st == 2 || __any(__builtin_fmaxf(L[0], L[1]) > 0x1p60f), 0)) {
            const int d0 = __builtin_amdgcn_frexp_expf(L[0]) + 30, d1 = __builtin_amdgcn_frexp_expf(L[1]) + 30;
            const float f0 = (float)d0, f1 = (float)d1;
            L[0] = __builtin_ldexpf(L[0], -d0); L[1] = __builtin_ldexpf(L[1], -d1);
#pragma unroll
            for (int r = 0; r < 16; ++r) { o0[r] = __builtin_ldexpf(o0[r], -d0); o1[r] = __builtin_ldexpf(o1[r], -d1); nm0[r] -= f0; nm1[r] -= f1; sA[r] -= f0; }
#pragma unroll
            for (int r = 0; r < 4; ++r) { pB0[r] = rescale_pk(pB0[r], -d1); pB1[r] = rescale_pk(pB1[r], -d1); }
        }
        const int so = (st & 3) * 16384, sn = ((st + 1) & 3) * 16384;
        KTILE(so, so + 4096); KTILE(so + 4096, so + 8192); KTILE(so + 8192, so + 12288); KTILE(so + 12288, sn);
    }
    o1 = MFMA32(vp0, BF(pB0), o1); L = MFMA16(sel1, BF(pB0), L); o1 = MFMA32(vp1, BF(pB1), o1); L = MFMA16(sel1, BF(pB1), L);
    float l0 = L[0], l1 = L[1];
    {
        float t = l0 + l1;
#pragma unroll
        for (int r = 0; r < 16; ++r) t += __builtin_fabsf(o0[r]) + __builtin_fabsf(o1[r]);
        if (__builtin_expect(__any(!(t < __builtin_inff()) || !(l0 > 0.f) || !(l1 > 0.f)), 0)) {
            float m0 = -__builtin_inff(), m1 = m0; l0 = 0.f; l1 = 0.f; o0 = f32x16{}; o1 = f32x16{};
            for (int kt = 0; kt < 64; ++kt) {
                const char* g = gkv + kt * 4096 + vl;
                const bf16x8 ka = *(const bf16x8*)g, kb = *(const bf16x8*)(g + 1024), va = *(const bf16x8*)(g + 2048), vb = *(const bf16x8*)(g + 3072);
                f32x16 z = {};
                f32x16 s0 = MFMA32(kb, q01, MFMA32(ka, q00, z)), s1 = MFMA32(kb, q11, MFMA32(ka, q10, z));
                const float n0 = __builtin_fmaxf(m0, rowmax16(s0)), n1 = __builtin_fmaxf(m1, rowmax16(s1));
                const float c0 = (n0 == -__builtin_inff()) ? 0.f : n0, c1 = (n1 == -__builtin_inff()) ? 0.f : n1;
                const float a0 = EX(m0 - c0), a1 = EX(m1 - c1); m0 = n0; m1 = n1;
#pragma unroll
                for (int r = 0; r < 16; ++r) { s0[r] = EX(s0[r] - c0); s1[r] = EX(s1[r] - c1); o0[r] *= a0; o1[r] *= a1; }
                const u32x4 a = pack8(s0, 0), bb = pack8(s0, 8), c = pack8(s1, 0), d = pack8(s1, 8);
                f32x4 ls = {}; ls = MFMA16(sel0, BF(a), ls); ls = MFMA16(sel0, BF(bb), ls); ls = MFMA16(sel1, BF(c), ls); ls = MFMA16(sel1, BF(d), ls);
                l0 = l0 * a0 + ls[0]; l1 = l1 * a1 + ls[1];
                o0 = MFMA32(vb, BF(bb), MFMA32(va, BF(a), o0)); o1 = MFMA32(vb, BF(d), MFMA32(va, BF(c), o1));
            }
        }
    }
    const float r0 = 1.0f / l0, r1 = 1.0f / l1;
    char* go = ws + WS_O + ((size_t)(tt0 * 32 + hd) << 11) + vl;
    *(u32x4*)go = pack8(o0, 0, r0); *(u32x4*)(go + 1024) = pack8(o0, 8, r0);
    *(u32x4*)(go + 65536) = pack8(o1, 0, r1); *(u32x4*)(go + 65536 + 1024) = pack8(o1, 8, r1);
#undef AT_DMA
#undef BODY
#undef KTILE
}

__device__ __forceinline__ void role_g4(int b, const char* wo, char* ws, char* out, char* lds) {
    const int lane = threadIdx.x & 63, wid = __builtin_amdgcn_readfirstlane(threadIdx.x >> 6);
    const int x = b & 7, i = b >> 3;
    const int tb = (x >> 1) * 4 + (i & 3), nb = (x & 1) * 8 + (i >> 2);
    const char* gw = wo + nb * 128;
    const char* gf = ws + WS_O + ((size_t)(tb * 4 + wid) << 16);
    unsigned vw[2];
#pragma unroll
    for (int j = 0; j < 2; ++j) { const int k = wid * 16 + 8 * j + (lane >> 3); vw[j] = (unsigned)(k * 2048 + (((lane & 7) ^ (((k >> 1) & 1) << 1)) << 4) - 1024 * j); }
    const unsigned vf = lane * 16;
    const unsigned ldw = (unsigned)(uintptr_t)lds + wid * 2048;
    const int r = lane & 31, h = lane >> 5, q4 = (lane >> 2) & 3, pp = lane & 3;
    const int ow = (4 * h + q4) * 128 + (((pp & 1) * 64 + ((lane >> 4) & 1) * 16 + (pp >> 1) * 8) ^ ((q4 >> 1) << 5));
    f32x16 acc[2] = {};
    bf16x8 f0[4], f1[4], f2[4], f3[4];
#define G4_ISSUE(k, slot, F) do { dma2(gw + (size_t)(k) * 64 * 2048, ldw + (slot) * 8192, vw[0], vw[1]); gld4(gf + (k) * 4096, vf, F[0], F[1], F[2], F[3]); } while (0)
#define G4_COMP(slot, F) do { \
        _Pragma("unroll") for (int kk = 0; kk < 4; ++kk) { const lds_cptr q_ = (lds_cptr)lds + (slot) * 8192 + kk * 2048; \
            const bf16x8 w0 = cat(ldstr(q_ + ow), ldstr(q_ + ow + 1024)), w1 = cat(ldstr(q_ + (ow ^ 32)), ldstr(q_ + (ow ^ 32) + 1024)); \
            acc[0] = MFMA32(w0, F[kk], acc[0]); acc[1] = MFMA32(w1, F[kk], acc[1]); } } while (0)
    G4_ISSUE(0, 0, f0); G4_ISSUE(1, 1, f1); G4_ISSUE(2, 2, f2);
    for (int k = 0; k < 12; k += 4) {
        WAIT_BAR4(12, f0); G4_ISSUE(k + 3, 3, f3); G4_COMP(0, f0);
        WAIT_BAR4(12, f1); G4_ISSUE(k + 4, 0, f0); G4_COMP(1, f1);
        WAIT_BAR4(12, f2); G4_ISSUE(k + 5, 1, f1); G4_COMP(2, f2);
        WAIT_BAR4(12, f3); G4_ISSUE(k + 6, 2, f2); G4_COMP(3, f3);
    }
    WAIT_BAR4(12, f0); G4_ISSUE(15, 3, f3); G4_COMP(0, f0);
    WAIT_BAR4(12, f1); G4_COMP(1, f1);
    WAIT_BAR4(6, f2); G4_COMP(2, f2);
    WAIT_BAR4(0, f3); G4_COMP(3, f3);
#undef G4_ISSUE
#undef G4_COMP
    char* d = out + (size_t)(tb * 128 + wid * 32 + r) * 4096 + 2048 + nb * 128 + h * 64;
    *(u32x4*)d = pack8(acc[0], 0); *(u32x4*)(d + 16) = pack8(acc[0], 8); *(u32x4*)(d + 32) = pack8(acc[1], 0); *(u32x4*)(d + 48) = pack8(acc[1], 8);
}

__device__ __forceinline__ unsigned sub_pk(unsigned a, unsigned b) {
    return cvtpk(__uint_as_float(a << 16) - __uint_as_float(b << 16), __uint_as_float(a & 0xffff0000u) - __uint_as_float(b & 0xffff0000u)); }
__device__ __forceinline__ void role_combine(int b, const char* ws, char* out) {
    u32x4 a[4], c[4];
#pragma unroll
    for (int j = 0; j < 4; ++j) { const int row = b * 8 + j * 2 + (threadIdx.x >> 7), col = (threadIdx.x & 127) * 16;
        a[j] = *(const u32x4*)(out + (size_t)row * 4096 + col); c[j] = *(const u32x4*)(ws + WS_PIM + (size_t)row * 2048 + col); }
#pragma unroll
    for (int j = 0; j < 4; ++j) { const int row = b * 8 + j * 2 + (threadIdx.x >> 7), col = (threadIdx.x & 127) * 16;
        *(u32x4*)(out + (size_t)row * 4096 + col) = (u32x4){sub_pk(a[j][0], c[j][0]), sub_pk(a[j][1], c[j][1]), sub_pk(a[j][2], c[j][2]), sub_pk(a[j][3], c[j][3])}; }
}

__global__ __launch_bounds__(256, 2) void k_phase1(const char* xA, const char* wre, const char* wim, const char* xB, const char* wqkv, char* ws) {
    extern __shared__ __attribute__((aligned(16))) char lds[];
    if (blockIdx.x < 256) role_g2(blockIdx.x, xB, wqkv, ws, lds); else role_g1(blockIdx.x - 256, xA, wre, wim, ws, lds);
}
__global__ __launch_bounds__(256, 2) void k_phase2(const char* wsre, const char* wsim, char* ws, char* out) {
    extern __shared__ __attribute__((aligned(16))) char lds[];
    if (blockIdx.x < 256) role_att(blockIdx.x, ws, lds); else role_g3(blockIdx.x - 256, wsre, wsim, ws, out, lds);
}
__global__ __launch_bounds__(256, 2) void k_phase3(const char* wo, char* ws, char* out) {
    extern __shared__ __attribute__((aligned(16))) char lds[];
    if (blockIdx.x < 256) role_g4(blockIdx.x, wo, ws, out, lds); else role_combine(blockIdx.x - 256, ws, out);
}

extern "C" void kernel_launch(void* const* d_in, const int* in_sizes, int n_in, void* d_out, int out_size, void* d_ws, size_t ws_size, hipStream_t stream) {
    const char* xA = (const char*)d_in[0]; const char* wsre = (const char*)d_in[1]; const char* wsim = (const char*)d_in[2];
    const char* wre = (const char*)d_in[3]; const char* wim = (const char*)d_in[4];
    const char* xB = (const char*)d_in[5]; const char* wqkv = (const char*)d_in[6]; const char* wo = (const char*)d_in[7];
    char* ws = (char*)d_ws; char* out = (char*)d_out;
    (void)hipFuncSetAttribute((const void*)k_phase1, hipFuncAttributeMaxDynamicSharedMemorySize, LDS_BYTES);
    (void)hipFuncSetAttribute((const void*)k_phase2, hipFuncAttributeMaxDynamicSharedMemorySize, LDS_BYTES);
    k_phase1<<<512, 256, LDS_BYTES, stream>>>(xA, wre, wim, xB, wqkv, ws);
    k_phase2<<<512, 256, 65536, stream>>>(wsre, wsim, ws, out);
    k_phase3<<<512, 256, 32768, stream>>>(wo, ws, out);
}
```

```cpp
#include <hip/hip_runtime.h>
#include <hip/hip_bf16.h>

typedef short bf16x8 __attribute__((ext_vector_type(8)));
typedef short s16x4 __attribute__((ext_vector_type(4)));
typedef float f32x16 __attribute__((ext_vector_type(16)));
typedef float f32x4 __attribute__((ext_vector_type(4)));
typedef unsigned u32x4 __attribute__((ext_vector_type(4)));
#define LAS __attribute__((address_space(3)))
typedef LAS const char* lds_cptr;

#define MFMA32(a, b, c) __builtin_amdgcn_mfma_f32_32x32x16_bf16(a, b, c, 0, 0, 0)
#define MFMA16(a, b, c) __builtin_amdgcn_mfma_f32_16x16x32_bf16(a, b, c, 0, 0, 0)
#define SBAR() __builtin_amdgcn_sched_barrier(0)
#define PIN(x) asm volatile("" : "+v"(x))

constexpr int S_ = 2048, D_ = 1024;
constexpr int LDS_BYTES = 81920;
constexpr size_t WS_AB = 0;
constexpr size_t WS_Q = WS_AB + (8u << 20);
constexpr size_t WS_KV = WS_Q + (4u << 20);
constexpr size_t WS_O = WS_KV + (8u << 20);
constexpr size_t WS_PIM = WS_O + (4u << 20);

typedef float f32x2 __attribute__((ext_vector_type(2)));
typedef __bf16 bf16x2_t __attribute__((ext_vector_type(2)));
__device__ __forceinline__ unsigned cvtpk(float lo, float hi) { return __builtin_bit_cast(unsigned, __builtin_convertvector((f32x2){lo, hi}, bf16x2_t)); }
__device__ __forceinline__ u32x4 pack8(const f32x16& a, int o, float sc) {
    return (u32x4){cvtpk(a[o] * sc, a[o + 1] * sc), cvtpk(a[o + 2] * sc, a[o + 3] * sc), cvtpk(a[o + 4] * sc, a[o + 5] * sc), cvtpk(a[o + 6] * sc, a[o + 7] * sc)}; }
__device__ __forceinline__ u32x4 pack8(const f32x16& a, int o) {
    return (u32x4){cvtpk(a[o], a[o + 1]), cvtpk(a[o + 2], a[o + 3]), cvtpk(a[o + 4], a[o + 5]), cvtpk(a[o + 6], a[o + 7])}; }
__device__ __forceinline__ bf16x8 ldsr(lds_cptr p) { return *(const LAS bf16x8*)p; }
typedef short v4i16_t __attribute__((ext_vector_type(4)));
__device__ __forceinline__ s16x4 ldstr(lds_cptr p) { return __builtin_bit_cast(s16x4, __builtin_amdgcn_ds_read_tr16_b64_v4i16((LAS v4i16_t*)p)); }
__device__ __forceinline__ bf16x8 cat(s16x4 a, s16x4 b) { return (bf16x8){a[0], a[1], a[2], a[3], b[0], b[1], b[2], b[3]}; }

__device__ __forceinline__ u32x4 pack44(const f32x4& a, const f32x4& b) { return (u32x4){cvtpk(a[0], a[1]), cvtpk(a[2], a[3]), cvtpk(b[0], b[1]), cvtpk(b[2], b[3])}; }
__device__ __forceinline__ u32x4 pack44(const f32x4& a, const f32x4& b, float s) { return pack44(a * s, b * s); }

template <int OFF> __device__ __forceinline__ void dma1(const char* sbase, unsigned lds, unsigned v) {
    asm volatile("s_mov_b32 m0, %1\n\ts_nop 0\n\tglobal_load_lds_dwordx4 %2, %0 offset:%3" :: "s"(sbase), "s"(lds), "v"(v), "i"(OFF) : "memory"); }
__device__ __forceinline__ void dma4(const char* sbase, unsigned lds, unsigned v0, unsigned v1, unsigned v2, unsigned v3) {
    asm volatile("s_mov_b32 m0, %1\n\ts_nop 0\n\t"
                 "global_load_lds_dwordx4 %2, %0\n\t"
                 "global_load_lds_dwordx4 %3, %0 offset:1024\n\t"
                 "global_load_lds_dwordx4 %4, %0 offset:2048\n\t"
                 "global_load_lds_dwordx4 %5, %0 offset:3072"
                 :: "s"(sbase), "s"(lds), "v"(v0), "v"(v1), "v"(v2), "v"(v3) : "memory"); }
template <int OFF> __device__ __forceinline__ void gld1(const char* sbase, unsigned voff, bf16x8& a) {
    asm volatile("global_load_dwordx4 %0, %2, %1 offset:%3" : "=&v"(a) : "s"(sbase), "v"(voff), "i"(OFF) : "memory"); }
#define WAIT_BAR(N) asm volatile("s_waitcnt vmcnt(" #N ") lgkmcnt(0)\n\ts_barrier" ::: "memory")
#define WAIT_BAR4(N, R) asm volatile("s_waitcnt vmcnt(" #N ") lgkmcnt(0)\n\ts_barrier" : "+v"(R[0]), "+v"(R[1]), "+v"(R[2]), "+v"(R[3]) :: "memory")

__device__ __forceinline__ int fsw(int r) { return ((r >> 1) & 7) ^ (((r >> 4) & 1) * 6); }
__device__ __forceinline__ int tsw(int k) { return ((k >> 1) & 1) | ((((k >> 2) ^ (k >> 3)) & 1) << 1); }
__device__ __forceinline__ int perm32(int rho, int T) { return 8 * (rho >> 2) + 4 * T + (rho & 3); }
__device__ __forceinline__ int tokmap(int rho, int T) { return 16 * (rho >> 3) + 8 * T + (rho & 7); }
__device__ __forceinline__ unsigned krow_off(int row0, int j, int lane, int ld) {
    const int r = row0 + 8 * j + (lane >> 3);
    return (unsigned)(r * ld + (((lane & 7) ^ fsw(r)) << 4) - 1024 * j); }
__device__ __forceinline__ unsigned nrow_off(int k0, int j, int lane, int ld) {
    const int k = k0 + 8 * j + (lane >> 3);
    return (unsigned)(k * ld + (((lane & 7) ^ (tsw(k) << 1)) << 4) - 1024 * j); }
__device__ __forceinline__ int frag_off(int row, int g) { return row * 128 + ((g ^ fsw(row)) << 4); }

__device__ __forceinline__ void role_g1(int b, const char* xA, const char* wre, const char* wim, char* ws, char* lds) {
    const int lane = threadIdx.x & 63, wid = __builtin_amdgcn_readfirstlane(threadIdx.x >> 6);
    const int x = b & 7, i = b >> 3;
    const int sm = (x >> 2) * 8 + (i & 7), en = (x & 3) * 4 + (i >> 3);
    const int p = en >> 3, e0 = (en & 7) * 128, s0 = sm * 128;
    const char* ga = xA + (size_t)s0 * 2048;
    const char* gb = (p ? wim : wre) + (size_t)e0 * 2048;
    unsigned vo[4];
#pragma unroll
    for (int j = 0; j < 4; ++j) vo[j] = krow_off(wid * 32, j, lane, 2048);
    const unsigned ldw = (unsigned)(uintptr_t)lds + wid * 4096;
    const int wm = wid >> 1, wn = wid & 1, rho = lane & 15, g = lane >> 4;
    int oa[2], ob[2];
#pragma unroll
    for (int T = 0; T < 2; ++T) { oa[T] = frag_off(wm * 64 + perm32(rho, T), g); ob[T] = 16384 + frag_off(wn * 64 + perm32(rho, T), g); }
    f32x4 acc[4][4] = {};
    bf16x8 fa[2][4], fb[2][4];
    dma4(ga, ldw, vo[0], vo[1], vo[2], vo[3]); dma4(gb, ldw + 16384, vo[0], vo[1], vo[2], vo[3]);
#define G1_RD(RS, CUR, KK, n) do { if ((n) < 4) fa[RS][(n) & 3] = ldsr((lds_cptr)lds + (CUR) + (oa[(n) & 1] ^ ((KK) * 64)) + (((n) >> 1) & 1) * 4096); \
                                   else fb[RS][(n) & 3] = ldsr((lds_cptr)lds + (CUR) + (ob[(n) & 1] ^ ((KK) * 64)) + (((n) >> 1) & 1) * 4096); } while (0)
#define G1_HALF(RS, CUR, KK, RD, MM, DM, kn) do { \
        _Pragma("unroll") for (int n = 0; n < 8; ++n) { \
            const int rn = (n == 1) ? 4 : (n == 2) ? 5 : (n == 3) ? 6 : (n == 4) ? 7 : (n == 0) ? 0 : n - 4; \
            if (RD) G1_RD(RS, CUR, KK, rn); \
            if (MM) { acc[n >> 1][(n & 1) * 2] = MFMA16(fa[RS ^ 1][n >> 1], fb[RS ^ 1][(n & 1) * 2], acc[n >> 1][(n & 1) * 2]); \
                      acc[n >> 1][(n & 1) * 2 + 1] = MFMA16(fa[RS ^ 1][n >> 1], fb[RS ^ 1][(n & 1) * 2 + 1], acc[n >> 1][(n & 1) * 2 + 1]); } \
            if (DM) { const unsigned d_ = ldw + ((CUR) ^ 32768); \
                if (n == 0) dma1<0>(ga + (kn) * 128, d_, vo[0]); if (n == 1) dma1<0>(gb + (kn) * 128, d_ + 16384, vo[0]); \
                if (n == 2) dma1<1024>(ga + (kn) * 128, d_, vo[1]); if (n == 3) dma1<1024>(gb + (kn) * 128, d_ + 16384, vo[1]); \
                if (n == 4) dma1<2048>(ga + (kn) * 128, d_, vo[2]); if (n == 5) dma1<2048>(gb + (kn) * 128, d_ + 16384, vo[2]); \
                if (n == 6) dma1<3072>(ga + (kn) * 128, d_, vo[3]); if (n == 7) dma1<3072>(gb + (kn) * 128, d_ + 16384, vo[3]); } \
            SBAR(); } } while (0)
    WAIT_BAR(0); G1_HALF(0, 0, 0, true, false, true, 1); G1_HALF(1, 0, 1, true, true, false, 0);
    for (int kt = 1; kt < 15; kt += 2) {
        WAIT_BAR(0); G1_HALF(0, 32768, 0, true, true, true, kt + 1); G1_HALF(1, 32768, 1, true, true, false, 0);
        WAIT_BAR(0); G1_HALF(0, 0, 0, true, true, true, kt + 2); G1_HALF(1, 0, 1, true, true, false, 0);
    }
    WAIT_BAR(0); G1_HALF(0, 32768, 0, true, true, false, 0); G1_HALF(1, 32768, 1, true, true, false, 0);
    G1_HALF(0, 0, 0, false, true, false, 0);
#undef G1_RD
#undef G1_HALF
    char* ab = ws + WS_AB + lane * 16;
#pragma unroll
    for (int blk = 0; blk < 2; ++blk)
#pragma unroll
        for (int n = 0; n < 4; ++n) {
            const int eb = (e0 >> 5) + wn * 2 + (n >> 1), sb = (s0 >> 5) + wm * 2 + blk;
            *(u32x4*)(ab + ((size_t)(((p * 32 + eb) * 64 + sb) * 2 + (n & 1)) << 10)) = pack44(acc[2 * blk][n], acc[2 * blk + 1][n]);
        }
}

__device__ __forceinline__ void role_g2(int b, const char* xB, const char* wqkv, char* ws, char* lds) {
    const int lane = threadIdx.x & 63, wid = __builtin_amdgcn_readfirstlane(threadIdx.x >> 6);
    const int x = b & 7, i = b >> 3;
    const int tm = (x >> 2) * 8 + (i & 7), hp = (x & 3) * 4 + (i >> 3);
    const char* ga = xB + (size_t)tm * 128 * 2048;
    const char* gw = wqkv + hp * 128;
    unsigned vo[4], vw[2];
#pragma unroll
    for (int j = 0; j < 4; ++j) vo[j] = krow_off(wid * 32, j, lane, 2048);
#pragma unroll
    for (int j = 0; j < 2; ++j) vw[j] = nrow_off(wid * 16, j, lane, 6144);
    const unsigned lds0 = (unsigned)(uintptr_t)lds;
    const int wm = wid >> 1, wn = wid & 1, rho = lane & 15, g = lane >> 4, q4 = (lane >> 2) & 3;
    int oa[2];
#pragma unroll
    for (int T = 0; T < 2; ++T) oa[T] = frag_off(wm * 64 + tokmap(rho, T), g);
    const int ow = 16384 + (8 * g + q4) * 128 + (((2 * wn) ^ tsw(8 * g + q4)) << 5) + (lane & 3) * 8;
    f32x4 acc[4][6] = {};
    bf16x8 fa[2][4]; s16x4 wl[2][6], wh[2][6];
#define G2_DMA(n, kt, buf) do { const unsigned d_ = lds0 + (buf); const char* w_ = gw + (size_t)(kt) * 64 * 6144; const char* a_ = ga + (kt) * 128; \
        if ((n) == 0) dma1<0>(a_, d_ + wid * 4096, vo[0]); if ((n) == 1) dma1<0>(w_, d_ + 16384 + wid * 2048, vw[0]); if ((n) == 2) dma1<1024>(w_, d_ + 16384 + wid * 2048, vw[1]); \
        if ((n) == 3) dma1<1024>(a_, d_ + wid * 4096, vo[1]); if ((n) == 4) dma1<0>(w_ + 2048, d_ + 24576 + wid * 2048, vw[0]); if ((n) == 5) dma1<1024>(w_ + 2048, d_ + 24576 + wid * 2048, vw[1]); \
        if ((n) == 6) dma1<2048>(a_, d_ + wid * 4096, vo[2]); if ((n) == 7) dma1<0>(w_ + 4096, d_ + 32768 + wid * 2048, vw[0]); if ((n) == 8) dma1<1024>(w_ + 4096, d_ + 32768 + wid * 2048, vw[1]); \
        if ((n) == 9) dma1<3072>(a_, d_ + wid * 4096, vo[3]); } while (0)
#define G2_RDA(RS, CUR, KK, m) fa[RS][m] = ldsr((lds_cptr)lds + (CUR) + (oa[(m) & 1] ^ ((KK) * 64)) + ((m) >> 1) * 4096)
#define G2_RDW(RS, CUR, KK, c) do { const lds_cptr w_ = (lds_cptr)lds + (CUR) + ((c) >> 1) * 8192 + (KK) * 4096; \
        wl[RS][c] = ldstr(w_ + (ow ^ (((c) & 1) * 32))); wh[RS][c] = ldstr(w_ + (ow ^ (((c) & 1) * 32) ^ 64) + 512); } while (0)
#define G2_MM(S, m, c) do { const bf16x8 w_ = cat(wl[S][c], wh[S][c]); acc[m][c] = ((c) < 4) ? MFMA16(w_, fa[S][m], acc[m][c]) : MFMA16(fa[S][m], w_, acc[m][c]); } while (0)
#define G2_HALF(RS, CUR, KK, RD, MM, DM, kn) do { \
        _Pragma("unroll") for (int n = 0; n < 12; ++n) { \
            if (RD) { if (n == 0) { G2_RDW(RS, CUR, KK, 0); } else if (n == 1) { G2_RDA(RS, CUR, KK, 0); G2_RDA(RS, CUR, KK, 1); } else if (n == 2) { G2_RDA(RS, CUR, KK, 2); G2_RDA(RS, CUR, KK, 3); } \
                      else if (n < 8) { G2_RDW(RS, CUR, KK, (n < 8 ? n - 2 : 0)); } } \
            if (MM) { G2_MM(RS ^ 1, (n & 1) * 2, n >> 1); G2_MM(RS ^ 1, (n & 1) * 2 + 1, n >> 1); } \
            if (DM) G2_DMA(n, kn, 40960 - (CUR)); \
            SBAR(); } } while (0)
#pragma unroll
    for (int n = 0; n < 10; ++n) G2_DMA(n, 0, 0);
    WAIT_BAR(0); G2_HALF(0, 0, 0, true, false, true, 1); G2_HALF(1, 0, 1, true, true, false, 0);
    for (int kt = 1; kt < 15; kt += 2) {
        WAIT_BAR(0); G2_HALF(0, 40960, 0, true, true, true, kt + 1); G2_HALF(1, 40960, 1, true, true, false, 0);
        WAIT_BAR(0); G2_HALF(0, 0, 0, true, true, true, kt + 2); G2_HALF(1, 0, 1, true, true, false, 0);
    }
    WAIT_BAR(0); G2_HALF(0, 40960, 0, true, true, false, 0); G2_HALF(1, 40960, 1, true, true, false, 0);
    G2_HALF(0, 0, 0, false, true, false, 0);
#undef G2_DMA
#undef G2_RDA
#undef G2_RDW
#undef G2_MM
#undef G2_HALF
    const int hd = hp * 2 + wn;
    constexpr float QS = 0.17677669529663687f * 1.4426950408889634f;
    const int co = (g >> 1) * 1024 + (g & 1) * 512;
#pragma unroll
    for (int blk = 0; blk < 2; ++blk) {
        const int tt = tm * 4 + wm * 2 + blk;
        char* q = ws + WS_Q + ((size_t)(hd * 64 + tt) << 11) + co;
        char* kv = ws + WS_KV + ((size_t)(hd * 64 + tt) << 12) + co;
#pragma unroll
        for (int T = 0; T < 2; ++T) {
            const int to = tokmap(rho, T) * 16;
            *(u32x4*)(q + to) = pack44(acc[2 * blk + T][0], acc[2 * blk + T][1], QS);
            *(u32x4*)(kv + to) = pack44(acc[2 * blk + T][2], acc[2 * blk + T][3]);
            *(u32x4*)(kv + 2048 + T * 256 + rho * 16) = pack44(acc[2 * blk][4 + T], acc[2 * blk + 1][4 + T]);
        }
    }
}

__device__ __forceinline__ void role_g3(int b, const char* wsre, const char* wsim, char* ws, char* out, char* lds) {
    const int lane = threadIdx.x & 63, wid = __builtin_amdgcn_readfirstlane(threadIdx.x >> 6);
    const int x = b & 7, i = b >> 3;
    const int p = x >> 2, tb = ((x >> 1) & 1) * 8 + (i & 7), eb = ((x & 1) * 4 + (i >> 3)) * 4 + wid;
    const char* gt = (p ? wsim : wsre) + (size_t)tb * 128 * 4096;
    const char* gf = ws + WS_AB + ((size_t)(p * 32 + eb) << 17);
    unsigned vo[4];
#pragma unroll
    for (int j = 0; j < 4; ++j) vo[j] = krow_off(wid * 32, j, lane, 4096);
    const unsigned vf = lane * 16;
    const unsigned ldw = (unsigned)(uintptr_t)lds + wid * 4096;
    const int rho = lane & 15, g = lane >> 4;
    int ob[2];
#pragma unroll
    for (int T = 0; T < 2; ++T) ob[T] = frag_off(16 * T + rho, g);
    f32x4 acc[2][8] = {};
    bf16x8 f0[4], f1[4], f2[4], f3[4], ft[2][8];
#define G3_ISS(n, k, slot, F) do { const char* t_ = gt + (k) * 128; const char* f_ = gf + (k) * 4096; const unsigned d_ = ldw + (slot) * 16384; \
        if ((n) == 0) dma1<0>(t_, d_, vo[0]); if ((n) == 1) dma1<1024>(t_, d_, vo[1]); if ((n) == 2) dma1<2048>(t_, d_, vo[2]); if ((n) == 3) dma1<3072>(t_, d_, vo[3]); \
        if ((n) == 4) gld1<0>(f_, vf, F[0]); if ((n) == 5) gld1<1024>(f_, vf, F[1]); if ((n) == 6) gld1<2048>(f_, vf, F[2]); if ((n) == 7) gld1<3072>(f_, vf, F[3]); } while (0)
#define G3_HALF(RS, slot, KK, RD, MM, A0, A1, IS, I0, k, islot, F) do { \
        _Pragma("unroll") for (int n = 0; n < 8; ++n) { \
            if (RD) ft[RS][n] = ldsr((lds_cptr)lds + (slot) * 16384 + (ob[n & 1] ^ ((KK) * 64)) + (n >> 1) * 4096); \
            if (MM) { acc[0][n] = MFMA16(A0, ft[RS ^ 1][n], acc[0][n]); acc[1][n] = MFMA16(A1, ft[RS ^ 1][n], acc[1][n]); } \
            if ((IS) && (n & 1) == 0) G3_ISS((I0) + (n >> 1), k, islot, F); \
            SBAR(); } } while (0)
#define G3_STEP(W, slot, FC, FP, MM, IS, k, islot) do { WAIT_BAR4(W, FC); \
        G3_HALF(0, slot, 0, true, MM, FP[2], FP[3], IS, 0, k, islot, FP); G3_HALF(1, slot, 1, true, true, FC[0], FC[1], IS, 4, k, islot, FP); } while (0)
#pragma unroll
    for (int n = 0; n < 8; ++n) G3_ISS(n, 0, 0, f0);
#pragma unroll
    for (int n = 0; n < 8; ++n) G3_ISS(n, 1, 1, f1);
#pragma unroll
    for (int n = 0; n < 8; ++n) G3_ISS(n, 2, 2, f2);
    G3_STEP(16, 0, f0, f3, false, true, 3, 3);
    for (int k = 1; k < 29; k += 4) {
        G3_STEP(16, 1, f1, f0, true, true, k + 3, 0);
        G3_STEP(16, 2, f2, f1, true, true, k + 4, 1);
        G3_STEP(16, 3, f3, f2, true, true, k + 5, 2);
        G3_STEP(16, 0, f0, f3, true, true, k + 6, 3);
    }
    G3_STEP(16, 1, f1, f0, true, false, 0, 0);
    G3_STEP(8, 2, f2, f1, true, false, 0, 0);
    G3_STEP(0, 3, f3, f2, true, false, 0, 0);
    G3_HALF(0, 0, 0, false, true, f3[2], f3[3], false, 0, 0, 0, f0);
#undef G3_ISS
#undef G3_HALF
#undef G3_STEP
    char* dst = p ? ws + WS_PIM : out; const int pitch = p ? 2048 : 4096;
    dst += (size_t)(tb * 128 + rho) * pitch + eb * 64 + g * 16;
#pragma unroll
    for (int u = 0; u < 8; ++u) *(u32x4*)(dst + (size_t)u * 16 * pitch) = pack44(acc[0][u], acc[1][u]);
}

__device__ __forceinline__ float xhalf_max(float m) { auto rr = __builtin_amdgcn_permlane32_swap(__float_as_uint(m), __float_as_uint(m), false, false);
    return __builtin_fmaxf(__uint_as_float(rr[0]), __uint_as_float(rr[1])); }
__device__ __forceinline__ float xhalf_sum(float m) { auto rr = __builtin_amdgcn_permlane32_swap(__float_as_uint(m), __float_as_uint(m), false, false);
    return __uint_as_float(rr[0]) + __uint_as_float(rr[1]); }
__device__ __forceinline__ float rowmax16(const f32x16& s) {
    float a = __builtin_fmaxf(s[0], s[1]);
#pragma unroll
    for (int r = 2; r < 16; r += 2) a = __builtin_fmaxf(__builtin_fmaxf(a, s[r]), s[r + 1]);
    return xhalf_max(a); }
__device__ __forceinline__ float max16(const f32x16& s, float a) {
#pragma unroll
    for (int r = 0; r < 16; r += 2) a = __builtin_fmaxf(__builtin_fmaxf(a, s[r]), s[r + 1]);
    return a; }
constexpr int REF = 64;
__device__ __forceinline__ unsigned rescale_pk(unsigned w, int d) {
    return cvtpk(__builtin_ldexpf(__uint_as_float(w << 16), d), __builtin_ldexpf(__uint_as_float(w & 0xffff0000u), d)); }

__device__ __forceinline__ void role_att(int b, char* ws, char* lds) {
    const int lane = threadIdx.x & 63, wid = __builtin_amdgcn_readfirstlane(threadIdx.x >> 6);
    const int x = b & 7, i = b >> 3;
    const int hd = x * 4 + (i >> 3), tt0 = (i & 7) * 8 + wid * 2;
    const char* gkv = ws + WS_KV + ((size_t)hd << 18);
    const unsigned vl = lane * 16;
    const unsigned ldw = (unsigned)(uintptr_t)lds + wid * 4096;
#define AT_DMA(st) dma4(gkv + (st) * 16384 + wid * 4096, ldw + ((st) & 3) * 16384, vl, vl, vl, vl)
    AT_DMA(0); AT_DMA(1);
    const char* gq = ws + WS_Q + ((size_t)(hd * 64 + tt0) << 11) + vl;
    const bf16x8 q00 = *(const bf16x8*)gq, q01 = *(const bf16x8*)(gq + 1024), q10 = *(const bf16x8*)(gq + 2048), q11 = *(const bf16x8*)(gq + 3072);
    bf16x8 sel0, sel1;
    { const bool on = ((lane >> 2) & 1) == ((lane >> 4) & 1); const short v0 = (on && (lane & 3) == 0) ? (short)0x3F80 : (short)0, v1 = (on && (lane & 3) == 1) ? (short)0x3F80 : (short)0;
      sel0 = (bf16x8){v0, v0, v0, v0, v0, v0, v0, v0}; sel1 = (bf16x8){v1, v1, v1, v1, v1, v1, v1, v1}; PIN(sel0); PIN(sel1); }
    const lds_cptr lp = (lds_cptr)lds + vl;
    f32x16 o0 = {}, o1 = {}, nm0, nm1, sA, sB = {}; f32x4 L = {};
    u32x4 pA0 = {}, pA1 = {}, pB0 = {}, pB1 = {};
    bf16x8 k0, k1, kn0, kn1, v0, v1, vp0 = {}, vp1 = {};
    WAIT_BAR(0);
    k0 = ldsr(lp); k1 = ldsr(lp + 1024);
    {
        const f32x16 z = {}; float m0 = -__builtin_inff(), m1 = m0;
#pragma unroll
        for (int kt = 0; kt < 4; ++kt) { const bf16x8 ka = ldsr(lp + kt * 4096), kb = ldsr(lp + kt * 4096 + 1024);
            m0 = max16(MFMA32(kb, q01, MFMA32(ka, q00, z)), m0); m1 = max16(MFMA32(kb, q11, MFMA32(ka, q10, z)), m1); }
        m0 = xhalf_max(m0) + (float)REF; m1 = xhalf_max(m1) + (float)REF;
#pragma unroll
        for (int r = 0; r < 16; ++r) { nm0[r] = -m0; nm1[r] = -m1; }
        PIN(nm0); PIN(nm1);
    }
    sA = MFMA32(k1, q01, MFMA32(k0, q00, nm0));
#define EX(x) __builtin_amdgcn_exp2f(x)
#define BF(x) __builtin_bit_cast(bf16x8, x)
#define E_(S, i) do { S[i] = EX(S[i]); SBAR(); } while (0)
#define C_(P, j, S, i) do { P[j] = cvtpk(S[i], S[i + 1]); SBAR(); } while (0)
#define BODY(SC, SN, PC0, PC1, PP0, PP1, KA, KB, QA, QB, NM, VA, VB, OP, SEL, LD0, LD1) do { \
        E_(SC, 0); C_(PP1, 3, SN, 14); PIN(PP1); SBAR(); \
        SN = MFMA32(KA, QA, NM); SBAR(); LD0; SBAR(); E_(SC, 1); E_(SC, 2); E_(SC, 3); PIN(SC); SBAR(); \
        OP = MFMA32(VA, BF(PP0), OP); SBAR(); LD1; SBAR(); E_(SC, 4); C_(PC0, 0, SC, 0); E_(SC, 5); C_(PC0, 1, SC, 2); PIN(SC); PIN(PC0); SBAR(); \
        SN = MFMA32(KB, QB, SN); SBAR(); E_(SC, 6); E_(SC, 7); E_(SC, 8); PIN(SC); SBAR(); \
        OP = MFMA32(VB, BF(PP1), OP); SBAR(); E_(SC, 9); C_(PC0, 2, SC, 4); E_(SC, 10); C_(PC0, 3, SC, 6); PIN(SC); PIN(PC0); SBAR(); \
        L = MFMA16(SEL, BF(PP0), L); SBAR(); E_(SC, 11); C_(PC1, 0, SC, 8); E_(SC, 12); E_(SC, 13); PIN(SC); PIN(PC1); SBAR(); \
        L = MFMA16(SEL, BF(PP1), L); SBAR(); C_(PC1, 1, SC, 10); E_(SC, 14); C_(PC1, 2, SC, 12); E_(SC, 15); PIN(SC); PIN(PC1); SBAR(); \
    } while (0)
#define KTILE(KO, KN) do { \
        BODY(sA, sB, pA0, pA1, pB0, pB1, k0, k1, q10, q11, nm1, vp0, vp1, o1, sel1, (kn0 = ldsr(lp + (KN)), kn1 = ldsr(lp + (KN) + 1024)), (v0 = ldsr(lp + (KO) + 2048), v1 = ldsr(lp + (KO) + 3072))); \
        BODY(sB, sA, pB0, pB1, pA0, pA1, kn0, kn1, q00, q01, nm0, v0, v1, o0, sel0, (void)0, (void)0); \
        k0 = kn0; k1 = kn1; vp0 = v0; vp1 = v1; } while (0)
    for (int st = 0; st < 16; ++st) {
        if (st) WAIT_BAR(0);
        if (st + 2 < 16) AT_DMA(st + 2);
        if (__builtin_expect(st == 4 || __any(__builtin_fmaxf(L[0], L[1]) > 0x1p40f), 0)) {
            const int d0 = __builtin_amdgcn_frexp_expf(L[0]) + REF, d1 = __builtin_amdgcn_frexp_expf(L[1]) + REF;
            const float f0 = (float)d0, f1 = (float)d1;
            L[0] = __builtin_ldexpf(L[0], -d0); L[1] = __builtin_ldexpf(L[1], -d1);
#pragma unroll
            for (int r = 0; r < 16; ++r) { o0[r] = __builtin_ldexpf(o0[r], -d0); o1[r] = __builtin_ldexpf(o1[r], -d1); nm0[r] -= f0; nm1[r] -= f1; sA[r] -= f0; }
#pragma unroll
            for (int r = 0; r < 4; ++r) { pB0[r] = rescale_pk(pB0[r], -d1); pB1[r] = rescale_pk(pB1[r], -d1); }
            sB[14] = __builtin_ldexpf(sB[14], -d1); sB[15] = __builtin_ldexpf(sB[15], -d1);
        }
        const int so = (st & 3) * 16384, sn = ((st + 1) & 3) * 16384;
        KTILE(so, so + 4096); KTILE(so + 4096, so + 8192); KTILE(so + 8192, so + 12288); KTILE(so + 12288, sn);
    }
    pB1[3] = cvtpk(sB[14], sB[15]);
    o1 = MFMA32(vp0, BF(pB0), o1); L = MFMA16(sel1, BF(pB0), L); o1 = MFMA32(vp1, BF(pB1), o1); L = MFMA16(sel1, BF(pB1), L);
    float l0 = L[0], l1 = L[1];
    {
        float t0 = 0.f, t1 = 0.f;
#pragma unroll
        for (int r = 0; r < 16; ++r) { t0 += __builtin_fabsf(o0[r]); t1 += __builtin_fabsf(o1[r]); }
        if (__builtin_expect(__any(!(t0 + t1 + l0 + l1 < __builtin_inff()) || !(l0 > 0.f) || !(l1 > 0.f) || !(t0 > 0x1p-100f) || !(t1 > 0x1p-100f)), 0)) {
            float m0 = -__builtin_inff(), m1 = m0; l0 = 0.f; l1 = 0.f; o0 = f32x16{}; o1 = f32x16{};
            for (int kt = 0; kt < 64; ++kt) {
                const char* g = gkv + kt * 4096 + vl;
                const bf16x8 ka = *(const bf16x8*)g, kb = *(const bf16x8*)(g + 1024), va = *(const bf16x8*)(g + 2048), vb = *(const bf16x8*)(g + 3072);
                f32x16 z = {};
                f32x16 s0 = MFMA32(kb, q01, MFMA32(ka, q00, z)), s1 = MFMA32(kb, q11, MFMA32(ka, q10, z));
                const float n0 = __builtin_fmaxf(m0, rowmax16(s0)), n1 = __builtin_fmaxf(m1, rowmax16(s1));
                const float c0 = (n0 == -__builtin_inff()) ? 0.f : n0, c1 = (n1 == -__builtin_inff()) ? 0.f : n1;
                const float a0 = EX(m0 - c0), a1 = EX(m1 - c1); m0 = n0; m1 = n1;
#pragma unroll
                for (int r = 0; r < 16; ++r) { s0[r] = EX(s0[r] - c0); s1[r] = EX(s1[r] - c1); o0[r] *= a0; o1[r] *= a1; }
                const u32x4 a = pack8(s0, 0), bb = pack8(s0, 8), c = pack8(s1, 0), d = pack8(s1, 8);
                f32x4 ls = {}; ls = MFMA16(sel0, BF(a), ls); ls = MFMA16(sel0, BF(bb), ls); ls = MFMA16(sel1, BF(c), ls); ls = MFMA16(sel1, BF(d), ls);
                l0 = l0 * a0 + ls[0]; l1 = l1 * a1 + ls[1];
                o0 = MFMA32(vb, BF(bb), MFMA32(va, BF(a), o0)); o1 = MFMA32(vb, BF(d), MFMA32(va, BF(c), o1));
            }
        }
    }
    const float r0 = 1.0f / l0, r1 = 1.0f / l1;
    char* go = ws + WS_O + ((size_t)(tt0 * 32 + hd) << 11) + ((lane >> 4) & 1) * 1024 + (lane >> 5) * 256 + (lane & 15) * 16;
    *(u32x4*)go = pack8(o0, 0, r0); *(u32x4*)(go + 512) = pack8(o0, 8, r0);
    *(u32x4*)(go + 65536) = pack8(o1, 0, r1); *(u32x4*)(go + 65536 + 512) = pack8(o1, 8, r1);
#undef E_
#undef C_
#undef AT_DMA
#undef BODY
#undef KTILE
}

__device__ __forceinline__ void role_g4(int b, const char* wo, char* ws, char* out, char* lds) {
    const int lane = threadIdx.x & 63, wid = __builtin_amdgcn_readfirstlane(threadIdx.x >> 6);
    const int x = b & 7, i = b >> 3;
    const int tb = (x >> 1) * 4 + (i & 3), nb = (x & 1) * 8 + (i >> 2);
    const char* gw = wo + nb * 128;
    const char* gf = ws + WS_O + ((size_t)(tb * 4 + wid) << 16);
    unsigned vw[2];
#pragma unroll
    for (int j = 0; j < 2; ++j) vw[j] = nrow_off(wid * 16, j, lane, 2048);
    const unsigned vf = lane * 16;
    const unsigned ldw = (unsigned)(uintptr_t)lds + wid * 2048;
    const int rho = lane & 15, g = lane >> 4, q4 = (lane >> 2) & 3;
    const int k0 = 16 * (g >> 1) + 4 * (g & 1) + q4;
    const int ow = k0 * 128 + (tsw(k0) << 5) + (lane & 3) * 8;
    f32x4 acc[2][4] = {};
    bf16x8 f0[4], f1[4], f2[4], f3[4]; s16x4 wl[2][4], wh[2][4];
#define G4_ISS(n, k, slot, F) do { const char* w_ = gw + (size_t)(k) * 64 * 2048; const char* f_ = gf + (k) * 4096; const unsigned d_ = ldw + (slot) * 8192; \
        if ((n) == 0) dma1<0>(w_, d_, vw[0]); if ((n) == 1) dma1<1024>(w_, d_, vw[1]); \
        if ((n) == 4) gld1<0>(f_, vf, F[0]); if ((n) == 5) gld1<1024>(f_, vf, F[1]); if ((n) == 6) gld1<2048>(f_, vf, F[2]); if ((n) == 7) gld1<3072>(f_, vf, F[3]); } while (0)
#define G4_HALF(RS, slot, KK, RD, MM, B0, B1, IS, I0, k, islot, F) do { \
        _Pragma("unroll") for (int n = 0; n < 4; ++n) { \
            if (RD) { const lds_cptr w_ = (lds_cptr)lds + (slot) * 8192 + (KK) * 4096; wl[RS][n] = ldstr(w_ + (ow ^ (n * 32))); wh[RS][n] = ldstr(w_ + (ow ^ (n * 32) ^ 64) + 1024); } \
            if (MM) { const bf16x8 w_ = cat(wl[RS ^ 1][n], wh[RS ^ 1][n]); acc[0][n] = MFMA16(w_, B0, acc[0][n]); acc[1][n] = MFMA16(w_, B1, acc[1][n]); } \
            if (IS) G4_ISS((I0) + n, k, islot, F); \
            SBAR(); } } while (0)
#define G4_STEP(W, slot, FC, FP, MM, IS, k, islot) do { WAIT_BAR4(W, FC); \
        G4_HALF(0, slot, 0, true, MM, FP[2], FP[3], IS, 0, k, islot, FP); G4_HALF(1, slot, 1, true, true, FC[0], FC[1], IS, 4, k, islot, FP); } while (0)
#pragma unroll
    for (int n = 0; n < 8; ++n) G4_ISS(n, 0, 0, f0);
#pragma unroll
    for (int n = 0; n < 8; ++n) G4_ISS(n, 1, 1, f1);
#pragma unroll
    for (int n = 0; n < 8; ++n) G4_ISS(n, 2, 2, f2);
    G4_STEP(12, 0, f0, f3, false, true, 3, 3);
    for (int k = 1; k < 13; k += 4) {
        G4_STEP(12, 1, f1, f0, true, true, k + 3, 0);
        G4_STEP(12, 2, f2, f1, true, true, k + 4, 1);
        G4_STEP(12, 3, f3, f2, true, true, k + 5, 2);
        G4_STEP(12, 0, f0, f3, true, true, k + 6, 3);
    }
    G4_STEP(12, 1, f1, f0, true, false, 0, 0);
    G4_STEP(6, 2, f2, f1, true, false, 0, 0);
    G4_STEP(0, 3, f3, f2, true, false, 0, 0);
    G4_HALF(0, 0, 0, false, true, f3[2], f3[3], false, 0, 0, 0, f0);
#undef G4_ISS
#undef G4_HALF
#undef G4_STEP
    char* d = out + (size_t)(tb * 128 + wid * 32 + rho) * 4096 + 2048 + nb * 128 + g * 8;
#pragma unroll
    for (int u = 0; u < 2; ++u)
#pragma unroll
        for (int n = 0; n < 4; ++n) *(uint2*)(d + u * 16 * 4096 + n * 32) = make_uint2(cvtpk(acc[u][n][0], acc[u][n][1]), cvtpk(acc[u][n][2], acc[u][n][3]));
}

__device__ __forceinline__ unsigned sub_pk(unsigned a, unsigned b) {
    return cvtpk(__uint_as_float(a << 16) - __uint_as_float(b << 16), __uint_as_float(a & 0xffff0000u) - __uint_as_float(b & 0xffff0000u)); }
__device__ __forceinline__ void role_combine(int b, const char* ws, char* out) {
    u32x4 a[4], c[4];
#pragma unroll
    for (int j = 0; j < 4; ++j) { const int row = b * 8 + j * 2 + (threadIdx.x >> 7), col = (threadIdx.x & 127) * 16;
        a[j] = *(const u32x4*)(out + (size_t)row * 4096 + col); c[j] = *(const u32x4*)(ws + WS_PIM + (size_t)row * 2048 + col); }
#pragma unroll
    for (int j = 0; j < 4; ++j) { const int row = b * 8 + j * 2 + (threadIdx.x >> 7), col = (threadIdx.x & 127) * 16;
        *(u32x4*)(out + (size_t)row * 4096 + col) = (u32x4){sub_pk(a[j][0], c[j][0]), sub_pk(a[j][1], c[j][1]), sub_pk(a[j][2], c[j][2]), sub_pk(a[j][3], c[j][3])}; }
}

#define T_BEGIN()
#define T_END(k)
__global__ __launch_bounds__(256, 2) void k_phase1(const char* xA, const char* wre, const char* wim, const char* xB, const char* wqkv, char* ws) {
    extern __shared__ __attribute__((aligned(16))) char lds[];
    T_BEGIN();
    if (blockIdx.x < 256) role_g2(blockIdx.x, xB, wqkv, ws, lds); else role_g1(blockIdx.x - 256, xA, wre, wim, ws, lds);
    T_END(0);
}
__global__ __launch_bounds__(256, 2) void k_phase2(const char* wsre, const char* wsim, char* ws, char* out) {
    extern __shared__ __attribute__((aligned(16))) char lds[];
    T_BEGIN();
    if (blockIdx.x < 256) role_att(blockIdx.x, ws, lds); else role_g3(blockIdx.x - 256, wsre, wsim, ws, out, lds);
    T_END(1);
}
__global__ __launch_bounds__(256, 2) void k_phase3(const char* wo, char* ws, char* out) {
    extern __shared__ __attribute__((aligned(16))) char lds[];
    T_BEGIN();
    if (blockIdx.x < 256) role_g4(blockIdx.x, wo, ws, out, lds); else role_combine(blockIdx.x - 256, ws, out);
    T_END(2);
}

extern "C" void kernel_launch(void* const* d_in, const int* in_sizes, int n_in, void* d_out, int out_size, void* d_ws, size_t ws_size, hipStream_t stream) {
    const char* xA = (const char*)d_in[0]; const char* wsre = (const char*)d_in[1]; const char* wsim = (const char*)d_in[2];
    const char* wre = (const char*)d_in[3]; const char* wim = (const char*)d_in[4];
    const char* xB = (const char*)d_in[5]; const char* wqkv = (const char*)d_in[6]; const char* wo = (const char*)d_in[7];
    char* ws = (char*)d_ws; char* out = (char*)d_out;
    (void)hipFuncSetAttribute((const void*)k_phase1, hipFuncAttributeMaxDynamicSharedMemorySize, LDS_BYTES);
    (void)hipFuncSetAttribute((const void*)k_phase2, hipFuncAttributeMaxDynamicSharedMemorySize, LDS_BYTES);
    k_phase1<<<512, 256, LDS_BYTES, stream>>>(xA, wre, wim, xB, wqkv, ws);
    k_phase2<<<512, 256, 65536, stream>>>(wsre, wsim, ws, out);
    k_phase3<<<512, 256, 32768, stream>>>(wo, ws, out);
}
```

```cpp
#include <hip/hip_runtime.h>
#include <hip/hip_bf16.h>

typedef short bf16x8 __attribute__((ext_vector_type(8)));
typedef short s16x4 __attribute__((ext_vector_type(4)));
typedef float f32x16 __attribute__((ext_vector_type(16)));
typedef float f32x4 __attribute__((ext_vector_type(4)));
typedef unsigned u32x4 __attribute__((ext_vector_type(4)));
#define LAS __attribute__((address_space(3)))
typedef LAS const char* lds_cptr;

#define MFMA32(a, b, c) __builtin_amdgcn_mfma_f32_32x32x16_bf16(a, b, c, 0, 0, 0)
#define MFMA16(a, b, c) __builtin_amdgcn_mfma_f32_16x16x32_bf16(a, b, c, 0, 0, 0)
#define SBAR() __builtin_amdgcn_sched_barrier(0)
#define PIN(x) asm volatile("" : "+v"(x))

constexpr int S_ = 2048, D_ = 1024;
constexpr int LDS_BYTES = 81920;
constexpr size_t WS_AB = 0;
constexpr size_t WS_Q = WS_AB + (8u << 20);
constexpr size_t WS_KV = WS_Q + (4u << 20);
constexpr size_t WS_O = WS_KV + (8u << 20);
constexpr size_t WS_PX = WS_O + (4u << 20);
constexpr size_t WS_FLAG = WS_PX + (4u << 20);
constexpr unsigned long long MAGIC = 0x9E3779B97F4A7C15ull;
typedef __attribute__((address_space(1))) unsigned long long gu64;

typedef float f32x2 __attribute__((ext_vector_type(2)));
typedef __bf16 bf16x2_t __attribute__((ext_vector_type(2)));
__device__ __forceinline__ unsigned cvtpk(float lo, float hi) { return __builtin_bit_cast(unsigned, __builtin_convertvector((f32x2){lo, hi}, bf16x2_t)); }
__device__ __forceinline__ u32x4 pack8(const f32x16& a, int o, float sc) {
    return (u32x4){cvtpk(a[o] * sc, a[o + 1] * sc), cvtpk(a[o + 2] * sc, a[o + 3] * sc), cvtpk(a[o + 4] * sc, a[o + 5] * sc), cvtpk(a[o + 6] * sc, a[o + 7] * sc)}; }
__device__ __forceinline__ u32x4 pack8(const f32x16& a, int o) {
    return (u32x4){cvtpk(a[o], a[o + 1]), cvtpk(a[o + 2], a[o + 3]), cvtpk(a[o + 4], a[o + 5]), cvtpk(a[o + 6], a[o + 7])}; }
__device__ __forceinline__ bf16x8 ldsr(lds_cptr p) { return *(const LAS bf16x8*)p; }
typedef short v4i16_t __attribute__((ext_vector_type(4)));
__device__ __forceinline__ s16x4 ldstr(lds_cptr p) { return __builtin_bit_cast(s16x4, __builtin_amdgcn_ds_read_tr16_b64_v4i16((LAS v4i16_t*)p)); }
__device__ __forceinline__ bf16x8 cat(s16x4 a, s16x4 b) { return (bf16x8){a[0], a[1], a[2], a[3], b[0], b[1], b[2], b[3]}; }

#define T_STAMP(k)
__device__ __forceinline__ u32x4 pack44(const f32x4& a, const f32x4& b) { return (u32x4){cvtpk(a[0], a[1]), cvtpk(a[2], a[3]), cvtpk(b[0], b[1]), cvtpk(b[2], b[3])}; }
__device__ __forceinline__ u32x4 pack44(const f32x4& a, const f32x4& b, float s) { return pack44(a * s, b * s); }

template <int OFF> __device__ __forceinline__ void dma1(const char* sbase, unsigned lds, unsigned v) {
    asm volatile("s_mov_b32 m0, %1\n\ts_nop 0\n\tglobal_load_lds_dwordx4 %2, %0 offset:%3" :: "s"(sbase), "s"(lds), "v"(v), "i"(OFF) : "memory"); }
template <int OFF> __device__ __forceinline__ void dma1nt(const char* sbase, unsigned lds, unsigned v) {
    asm volatile("s_mov_b32 m0, %1\n\ts_nop 0\n\tglobal_load_lds_dwordx4 %2, %0 offset:%3 nt" :: "s"(sbase), "s"(lds), "v"(v), "i"(OFF) : "memory"); }
__device__ __forceinline__ void dma4(const char* sbase, unsigned lds, unsigned v0, unsigned v1, unsigned v2, unsigned v3) {
    asm volatile("s_mov_b32 m0, %1\n\ts_nop 0\n\t"
                 "global_load_lds_dwordx4 %2, %0\n\t"
                 "global_load_lds_dwordx4 %3, %0 offset:1024\n\t"
                 "global_load_lds_dwordx4 %4, %0 offset:2048\n\t"
                 "global_load_lds_dwordx4 %5, %0 offset:3072"
                 :: "s"(sbase), "s"(lds), "v"(v0), "v"(v1), "v"(v2), "v"(v3) : "memory"); }
#define GLD_SC " sc1"
#define GLD_ACQ false
__device__ __forceinline__ void dma4dev(const char* sbase, unsigned lds, unsigned v) {
    asm volatile("s_mov_b32 m0, %1\n\ts_nop 0\n\t"
                 "global_load_lds_dwordx4 %2, %0" GLD_SC "\n\t"
                 "global_load_lds_dwordx4 %2, %0 offset:1024" GLD_SC "\n\t"
                 "global_load_lds_dwordx4 %2, %0 offset:2048" GLD_SC "\n\t"
                 "global_load_lds_dwordx4 %2, %0 offset:3072" GLD_SC
                 :: "s"(sbase), "s"(lds), "v"(v) : "memory"); }
__device__ __forceinline__ bf16x8 ld_dev(char* ws, unsigned off) {
    return __builtin_bit_cast(bf16x8, __builtin_amdgcn_raw_buffer_load_b128(__builtin_amdgcn_make_buffer_rsrc(ws, 0, 0x7fffffff, 0x00020000), off, 0, 16)); }
template <int OFF> __device__ __forceinline__ void gld1(const char* sbase, unsigned voff, bf16x8& a) {
    asm volatile("global_load_dwordx4 %0, %2, %1 offset:%3" GLD_SC : "=&v"(a) : "s"(sbase), "v"(voff), "i"(OFF) : "memory"); }
__device__ __forceinline__ void touch(const char* sbase, unsigned voff, unsigned& sink) {
    asm volatile("global_load_dword %0, %2, %1" : "+v"(sink) : "s"(sbase), "v"(voff) : "memory"); }
#define WAIT_BAR(N) do { __builtin_amdgcn_s_waitcnt(0xC07F); asm volatile("s_waitcnt vmcnt(" #N ")\n\ts_barrier" ::: "memory"); } while (0)
#define WAIT_BAR4(N, R) do { __builtin_amdgcn_s_waitcnt(0xC07F); asm volatile("s_waitcnt vmcnt(" #N ")\n\ts_barrier" : "+v"(R[0]), "+v"(R[1]), "+v"(R[2]), "+v"(R[3]) :: "memory"); } while (0)

__device__ __forceinline__ void mma16(f32x4& c, const bf16x8& a, const bf16x8& b) { asm volatile("v_mfma_f32_16x16x32_bf16 %0, %1, %2, %0" : "+v"(c) : "v"(a), "v"(b)); }
#define MMA_DRAIN() do { SBAR(); asm volatile("s_nop 7\n\ts_nop 7"); SBAR(); } while (0)
__device__ __forceinline__ void st_wt(char* ws, unsigned off, u32x4 v) {
    __builtin_amdgcn_raw_buffer_store_b128(v, __builtin_amdgcn_make_buffer_rsrc(ws, 0, 0x7fffffff, 0x00020000), off, 0, 16); }
__device__ __forceinline__ gu64* flag_at(char* ws, int kind, int consumer, int slot) { return (gu64*)(ws + WS_FLAG) + ((kind * 256 + consumer) * 32 + slot); }
__device__ __forceinline__ void publish(char* ws, int kind, int n, int consumer, int slot) {
    asm volatile("s_waitcnt vmcnt(0) lgkmcnt(0)\n\ts_barrier" ::: "memory");
    if (threadIdx.x < n) __hip_atomic_store(flag_at(ws, kind, consumer, slot), MAGIC, __ATOMIC_RELAXED, __HIP_MEMORY_SCOPE_AGENT);
}
#define POLL_NAP 4
template <bool ACQ, int NAP = POLL_NAP> __device__ __forceinline__ void await(char* ws, int kind, int n, int me, int slot0 = 0) {
    if (__builtin_amdgcn_readfirstlane(threadIdx.x) == 0) {
        gu64* f = flag_at(ws, kind, me, slot0 + (threadIdx.x & (n - 1)));
        const unsigned long long t0 = __builtin_amdgcn_s_memrealtime();
        while (!__all(__hip_atomic_load(f, __ATOMIC_RELAXED, __HIP_MEMORY_SCOPE_AGENT) == MAGIC)) {
            if (__builtin_amdgcn_s_memrealtime() - t0 > 20000000ull) break;
            __builtin_amdgcn_s_sleep(NAP); }
        if (threadIdx.x < n) __hip_atomic_store(f, 0ull, __ATOMIC_RELAXED, __HIP_MEMORY_SCOPE_AGENT);
        if (ACQ) { __builtin_amdgcn_fence(__ATOMIC_ACQUIRE, "agent"); asm volatile("s_waitcnt vmcnt(0)" ::: "memory"); }
    }
    asm volatile("s_barrier" ::: "memory");
}

__device__ __forceinline__ int fsw(int r) { return ((r >> 1) & 7) ^ (((r >> 4) & 1) * 6); }
__device__ __forceinline__ int tsw(int k) { return ((k >> 1) & 1) | ((((k >> 2) ^ (k >> 3)) & 1) << 1); }
__device__ __forceinline__ int perm32(int rho, int T) { return 8 * (rho >> 2) + 4 * T + (rho & 3); }
__device__ __forceinline__ int tokmap(int rho, int T) { return 16 * (rho >> 3) + 8 * T + (rho & 7); }
__device__ __forceinline__ unsigned krow_off(int row0, int j, int lane, int ld) {
    const int r = row0 + 8 * j + (lane >> 3);
    return (unsigned)(r * ld + (((lane & 7) ^ fsw(r)) << 4) - 1024 * j); }
__device__ __forceinline__ unsigned nrow_off(int k0, int j, int lane, int ld) {
    const int k = k0 + 8 * j + (lane >> 3);
    return (unsigned)(k * ld + (((lane & 7) ^ (tsw(k) << 1)) << 4) - 1024 * j); }
__device__ __forceinline__ int frag_off(int row, int g) { return row * 128 + ((g ^ fsw(row)) << 4); }

__device__ __forceinline__ int krot(int x, int i) { return 2 * (i & 7) + (x >> 2) + 4 * (i >> 3) + (x & 3); }
#define RB 4
__device__ __forceinline__ int krot0(int x, int i) { return RB == 16 ? krot(x, i) : RB == 8 ? (i & 7) + 2 * (i >> 3) : (i & 7) + (i >> 3); }
__device__ __forceinline__ int kidx(int k, int k0) { return (k & ~(RB - 1)) | ((k + k0) & (RB - 1)); }
__device__ __forceinline__ int g3_index(int p, int tb, int et) { return (p * 4 + (tb >> 2)) + 8 * ((tb & 3) + 4 * et); }
__device__ __forceinline__ int att_index(int hd, int qb) { return (hd >> 2) + 8 * ((hd & 3) * 8 + qb); }
__device__ __forceinline__ int g4_index(int tb, int nb) { return ((tb >> 2) * 2 + (nb >> 3)) + 8 * ((tb & 3) + 4 * (nb & 7)); }

__device__ __forceinline__ void role_g1(int b, const char* xA, const char* wre, const char* wim, char* ws, char* lds) {
    const int lane = threadIdx.x & 63, wid = __builtin_amdgcn_readfirstlane(threadIdx.x >> 6);
    const int x = b & 7, i = b >> 3;
    const int sm = (x >> 2) * 8 + (i & 7), en = (x & 3) * 4 + (i >> 3);
    const int p = en >> 3, e0 = (en & 7) * 128, s0 = sm * 128;
    const char* ga = xA + (size_t)s0 * 2048;
    const char* gb = (p ? wim : wre) + (size_t)e0 * 2048;
    unsigned vo[4];
#pragma unroll
    for (int j = 0; j < 4; ++j) vo[j] = krow_off(wid * 32, j, lane, 2048);
    const unsigned ldw = (unsigned)(uintptr_t)lds + wid * 4096;
    const int wm = wid >> 1, wn = wid & 1, rho = lane & 15, g = lane >> 4;
    int oa[2], ob[2];
#pragma unroll
    for (int T = 0; T < 2; ++T) { oa[T] = frag_off(wm * 64 + perm32(rho, T), g); ob[T] = 16384 + frag_off(wn * 64 + perm32(rho, T), g); }
    f32x4 acc[4][4] = {};
#pragma unroll
    for (int n = 0; n < 16; ++n) PIN(acc[n >> 2][n & 3]);
    bf16x8 fa[2][4], fb[2][4];
#define A_NAP 100
    __builtin_amdgcn_s_sleep(A_NAP);
    T_STAMP(9);
    const int k0 = krot0(x, i);
    dma4(ga + kidx(0, k0) * 128, ldw, vo[0], vo[1], vo[2], vo[3]); dma4(gb + kidx(0, k0) * 128, ldw + 16384, vo[0], vo[1], vo[2], vo[3]);
#define G1_RD(RS, CUR, KK, n) do { if ((n) < 4) fa[RS][(n) & 3] = ldsr((lds_cptr)lds + (CUR) + (oa[(n) & 1] ^ ((KK) * 64)) + (((n) >> 1) & 1) * 4096); \
                                   else fb[RS][(n) & 3] = ldsr((lds_cptr)lds + (CUR) + (ob[(n) & 1] ^ ((KK) * 64)) + (((n) >> 1) & 1) * 4096); } while (0)
#define G1_HALF(RS, CUR, KK, RD, MM, DM, kn) do { \
        _Pragma("unroll") for (int n = 0; n < 8; ++n) { \
            const int rn = (n == 1) ? 4 : (n == 2) ? 5 : (n == 3) ? 6 : (n == 4) ? 7 : (n == 0) ? 0 : n - 4; \
            if (RD) G1_RD(RS, CUR, KK, rn); \
            if (MM) { mma16(acc[n >> 1][(n & 1) * 2], fa[RS ^ 1][n >> 1], fb[RS ^ 1][(n & 1) * 2]); mma16(acc[n >> 1][(n & 1) * 2 + 1], fa[RS ^ 1][n >> 1], fb[RS ^ 1][(n & 1) * 2 + 1]); } \
            if (DM) { const unsigned d_ = ldw + ((CUR) ^ 32768); const int ko_ = kidx(kn, k0) * 128; \
                if (n == 0) dma1<0>(ga + ko_, d_, vo[0]); if (n == 1) dma1<0>(gb + ko_, d_ + 16384, vo[0]); \
                if (n == 2) dma1<1024>(ga + ko_, d_, vo[1]); if (n == 3) dma1<1024>(gb + ko_, d_ + 16384, vo[1]); \
                if (n == 4) dma1<2048>(ga + ko_, d_, vo[2]); if (n == 5) dma1<2048>(gb + ko_, d_ + 16384, vo[2]); \
                if (n == 6) dma1<3072>(ga + ko_, d_, vo[3]); if (n == 7) dma1<3072>(gb + ko_, d_ + 16384, vo[3]); } \
            SBAR(); } } while (0)
    WAIT_BAR(0); G1_HALF(0, 0, 0, true, false, true, 1); G1_HALF(1, 0, 1, true, true, false, 0);
    for (int kt = 1; kt < 15; kt += 2) {
        WAIT_BAR(0); G1_HALF(0, 32768, 0, true, true, true, kt + 1); G1_HALF(1, 32768, 1, true, true, false, 0);
        WAIT_BAR(0); G1_HALF(0, 0, 0, true, true, true, kt + 2); G1_HALF(1, 0, 1, true, true, false, 0);
    }
    WAIT_BAR(0); G1_HALF(0, 32768, 0, true, true, false, 0); G1_HALF(1, 32768, 1, true, true, false, 0);
    G1_HALF(0, 0, 0, false, true, false, 0);
#undef G1_RD
#undef G1_HALF
    MMA_DRAIN(); T_STAMP(1);
#pragma unroll
    for (int blk = 0; blk < 2; ++blk)
#pragma unroll
        for (int n = 0; n < 4; ++n) {
            const int eb = (e0 >> 5) + wn * 2 + (n >> 1), sb = (s0 >> 5) + wm * 2 + blk;
            st_wt(ws, (unsigned)WS_AB + ((((p * 32 + eb) * 64 + sb) * 2 + (n & 1)) << 10) + lane * 16, pack44(acc[2 * blk][n], acc[2 * blk + 1][n]));
        }
    publish(ws, 1, 16, g3_index(p, lane & 15, en & 7), sm);
}

__device__ __forceinline__ void role_g2(int b, const char* xB, const char* wqkv, char* ws, char* lds) {
    const int lane = threadIdx.x & 63, wid = __builtin_amdgcn_readfirstlane(threadIdx.x >> 6);
    const int x = b & 7, i = b >> 3;
    const int tm = (x >> 2) * 8 + (i & 7), hp = (x & 3) * 4 + (i >> 3);
    const char* ga = xB + (size_t)tm * 128 * 2048;
    const char* gw = wqkv + hp * 128;
    unsigned vo[4], vw[2];
#pragma unroll
    for (int j = 0; j < 4; ++j) vo[j] = krow_off(wid * 32, j, lane, 2048);
#pragma unroll
    for (int j = 0; j < 2; ++j) vw[j] = nrow_off(wid * 16, j, lane, 6144);
    const unsigned lds0 = (unsigned)(uintptr_t)lds;
    const int wm = wid >> 1, wn = wid & 1, rho = lane & 15, g = lane >> 4, q4 = (lane >> 2) & 3;
    int oa[2];
#pragma unroll
    for (int T = 0; T < 2; ++T) oa[T] = frag_off(wm * 64 + tokmap(rho, T), g);
    const int ow = 16384 + (8 * g + q4) * 128 + (((2 * wn) ^ tsw(8 * g + q4)) << 5) + (lane & 3) * 8;
    f32x4 acc[4][6] = {};
#pragma unroll
    for (int n = 0; n < 24; ++n) PIN(acc[n / 6][n % 6]);
    bf16x8 fa[2][4]; s16x4 wl[2][6], wh[2][6];
    const int k0 = krot0(x, i);
#define G2_DMA(n, kt, buf) do { const unsigned d_ = lds0 + (buf); const int kr_ = kidx(kt, k0); const char* w_ = gw + kr_ * (64 * 6144); const char* a_ = ga + kr_ * 128; \
        if ((n) == 0) dma1<0>(a_, d_ + wid * 4096, vo[0]); if ((n) == 1) dma1<0>(w_, d_ + 16384 + wid * 2048, vw[0]); if ((n) == 2) dma1<1024>(w_, d_ + 16384 + wid * 2048, vw[1]); \
        if ((n) == 3) dma1<1024>(a_, d_ + wid * 4096, vo[1]); if ((n) == 4) dma1<0>(w_ + 2048, d_ + 24576 + wid * 2048, vw[0]); if ((n) == 5) dma1<1024>(w_ + 2048, d_ + 24576 + wid * 2048, vw[1]); \
        if ((n) == 6) dma1<2048>(a_, d_ + wid * 4096, vo[2]); if ((n) == 7) dma1<0>(w_ + 4096, d_ + 32768 + wid * 2048, vw[0]); if ((n) == 8) dma1<1024>(w_ + 4096, d_ + 32768 + wid * 2048, vw[1]); \
        if ((n) == 9) dma1<3072>(a_, d_ + wid * 4096, vo[3]); } while (0)
#define G2_RDA(RS, CUR, KK, m) fa[RS][m] = ldsr((lds_cptr)lds + (CUR) + (oa[(m) & 1] ^ ((KK) * 64)) + ((m) >> 1) * 4096)
#define G2_RDW(RS, CUR, KK, c) do { const lds_cptr w_ = (lds_cptr)lds + (CUR) + ((c) >> 1) * 8192 + (KK) * 4096; \
        wl[RS][c] = ldstr(w_ + (ow ^ (((c) & 1) * 32))); wh[RS][c] = ldstr(w_ + (ow ^ (((c) & 1) * 32) ^ 64) + 512); } while (0)
#define G2_MM(S, m, c) do { const bf16x8 w_ = cat(wl[S][c], wh[S][c]); if ((c) < 4) mma16(acc[m][c], w_, fa[S][m]); else mma16(acc[m][c], fa[S][m], w_); } while (0)
#define G2_HALF(RS, CUR, KK, RD, MM, DM, kn) do { \
        _Pragma("unroll") for (int n = 0; n < 12; ++n) { \
            if (RD) { if (n == 0) { G2_RDW(RS, CUR, KK, 0); } else if (n == 1) { G2_RDA(RS, CUR, KK, 0); G2_RDA(RS, CUR, KK, 1); } else if (n == 2) { G2_RDA(RS, CUR, KK, 2); G2_RDA(RS, CUR, KK, 3); } \
                      else if (n < 8) { G2_RDW(RS, CUR, KK, (n < 8 ? n - 2 : 0)); } } \
            if (MM) { G2_MM(RS ^ 1, (n & 1) * 2, n >> 1); G2_MM(RS ^ 1, (n & 1) * 2 + 1, n >> 1); } \
            if (DM) G2_DMA(n, kn, 40960 - (CUR)); \
            SBAR(); } } while (0)
#pragma unroll
    for (int n = 0; n < 10; ++n) G2_DMA(n, 0, 0);
    WAIT_BAR(0); G2_HALF(0, 0, 0, true, false, true, 1); G2_HALF(1, 0, 1, true, true, false, 0);
    for (int kt = 1; kt < 15; kt += 2) {
        WAIT_BAR(0); G2_HALF(0, 40960, 0, true, true, true, kt + 1); G2_HALF(1, 40960, 1, true, true, false, 0);
        WAIT_BAR(0); G2_HALF(0, 0, 0, true, true, true, kt + 2); G2_HALF(1, 0, 1, true, true, false, 0);
    }
    WAIT_BAR(0);
    G2_HALF(0, 40960, 0, true, true, false, 0); G2_HALF(1, 40960, 1, true, true, false, 0);
    G2_HALF(0, 0, 0, false, true, false, 0);
#undef G2_DMA
#undef G2_RDA
#undef G2_RDW
#undef G2_MM
#undef G2_HALF
    MMA_DRAIN(); T_STAMP(1);
    const int hd = hp * 2 + wn;
    constexpr float QS = 0.17677669529663687f * 1.4426950408889634f;
    const int co = (g >> 1) * 1024 + (g & 1) * 512;
#pragma unroll
    for (int blk = 0; blk < 2; ++blk) {
        const int tt = tm * 4 + wm * 2 + blk;
        const unsigned q = (unsigned)WS_Q + ((hd * 64 + tt) << 11) + co, kv = (unsigned)WS_KV + ((hd * 64 + tt) << 12) + co;
#pragma unroll
        for (int T = 0; T < 2; ++T) {
            const int to = tokmap(rho, T) * 16;
            st_wt(ws, kv + to, pack44(acc[2 * blk + T][2], acc[2 * blk + T][3]));
            st_wt(ws, kv + 2048 + T * 256 + rho * 16, pack44(acc[2 * blk][4 + T], acc[2 * blk + 1][4 + T]));
            st_wt(ws, q + to, pack44(acc[2 * blk + T][0], acc[2 * blk + T][1], QS));
        }
    }
    publish(ws, 0, 16, att_index(hp * 2 + ((lane >> 3) & 1), lane & 7), tm);
}

__device__ __forceinline__ unsigned sub_pk(unsigned a, unsigned b) {
    return cvtpk(__uint_as_float(a << 16) - __uint_as_float(b << 16), __uint_as_float(a & 0xffff0000u) - __uint_as_float(b & 0xffff0000u)); }
#define G3DMA dma1
__device__ __forceinline__ void role_g3(int b, const char* wsre, const char* wsim, const char* wo, char* ws, char* out, char* lds) {
    const int lane = threadIdx.x & 63, wid = __builtin_amdgcn_readfirstlane(threadIdx.x >> 6);
    const int x = b & 7, i = b >> 3;
    const int p = x >> 2, tb = (x & 3) * 4 + (i & 3), eb = (i >> 2) * 4 + wid;
    const char* gt = (p ? wsim : wsre) + (size_t)tb * 128 * 4096;
    const char* gf = ws + WS_AB + ((size_t)(p * 32 + eb) << 17);
    unsigned vo[4];
#pragma unroll
    for (int j = 0; j < 4; ++j) vo[j] = krow_off(wid * 32, j, lane, 4096);
    const unsigned vf = lane * 16;
    const unsigned ldw = (unsigned)(uintptr_t)lds + wid * 4096;
    const int rho = lane & 15, g = lane >> 4;
    int ob[2];
#pragma unroll
    for (int T = 0; T < 2; ++T) ob[T] = frag_off(16 * T + rho, g);
    f32x4 acc[2][8] = {};
#pragma unroll
    for (int n = 0; n < 16; ++n) PIN(acc[n >> 3][n & 7]);
    bf16x8 f0[4], f1[4], f2[4], f3[4], ft[2][8];
    const int kr0 = (i >> 2) + 2 * (i & 3);
#define G3_ISS(n, k, slot, F) do { const int kr_ = ((k) & ~7) | (((k) + kr0) & 7); const char* t_ = gt + kr_ * 128; const char* f_ = gf + kr_ * 4096; const unsigned d_ = ldw + (slot) * 16384; \
        if ((n) == 0) G3DMA<0>(t_, d_, vo[0]); if ((n) == 1) G3DMA<1024>(t_, d_, vo[1]); if ((n) == 2) G3DMA<2048>(t_, d_, vo[2]); if ((n) == 3) G3DMA<3072>(t_, d_, vo[3]); \
        if ((n) == 4) gld1<0>(f_, vf, F[0]); if ((n) == 5) gld1<1024>(f_, vf, F[1]); if ((n) == 6) gld1<2048>(f_, vf, F[2]); if ((n) == 7) gld1<3072>(f_, vf, F[3]); } while (0)
#define G3_HALF(RS, slot, KK, RD, MM, A0, A1, IS, I0, k, islot, F) do { \
        _Pragma("unroll") for (int n = 0; n < 8; ++n) { \
            if (RD) ft[RS][n] = ldsr((lds_cptr)lds + (slot) * 16384 + (ob[n & 1] ^ ((KK) * 64)) + (n >> 1) * 4096); \
            if (MM) { mma16(acc[0][n], A0, ft[RS ^ 1][n]); mma16(acc[1][n], A1, ft[RS ^ 1][n]); } \
            if ((IS) && (n & 1) == 0) G3_ISS((I0) + (n >> 1), k, islot, F); \
            SBAR(); } } while (0)
#define G3_STEP(W, slot, FC, FP, MM, IS, k, islot) do { WAIT_BAR4(W, FC); \
        G3_HALF(0, slot, 0, true, MM, FP[2], FP[3], IS, 0, k, islot, FP); G3_HALF(1, slot, 1, true, true, FC[0], FC[1], IS, 4, k, islot, FP); } while (0)
#pragma unroll
    for (int n = 0; n < 12; ++n) G3_ISS(n & 3, n >> 2, n >> 2, f0);
    { unsigned sink = 0; touch(wo + (b * 4 + wid) * 2048, (lane & 15) * 128, sink); asm volatile("s_waitcnt vmcnt(0)" : "+v"(sink) :: "memory"); }
    await<GLD_ACQ>(ws, 1, 16, b); T_STAMP(3);
#pragma unroll
    for (int n = 4; n < 8; ++n) G3_ISS(n, 0, 0, f0);
#pragma unroll
    for (int n = 4; n < 8; ++n) G3_ISS(n, 1, 1, f1);
#pragma unroll
    for (int n = 4; n < 8; ++n) G3_ISS(n, 2, 2, f2);
    G3_STEP(8, 0, f0, f3, false, true, 3, 3);
    G3_STEP(12, 1, f1, f0, true, true, 4, 0);
    G3_STEP(16, 2, f2, f1, true, true, 5, 1);
    G3_STEP(16, 3, f3, f2, true, true, 6, 2);
    G3_STEP(16, 0, f0, f3, true, true, 7, 3);
    for (int k = 5; k < 29; k += 4) {
        G3_STEP(16, 1, f1, f0, true, true, k + 3, 0);
        G3_STEP(16, 2, f2, f1, true, true, k + 4, 1);
        G3_STEP(16, 3, f3, f2, true, true, k + 5, 2);
        G3_STEP(16, 0, f0, f3, true, true, k + 6, 3);
    }
    G3_STEP(16, 1, f1, f0, true, false, 0, 0);
    G3_STEP(8, 2, f2, f1, true, false, 0, 0);
    G3_STEP(0, 3, f3, f2, true, false, 0, 0);
    G3_HALF(0, 0, 0, false, true, f3[2], f3[3], false, 0, 0, 0, f0);
#undef G3_ISS
#undef G3_HALF
#undef G3_STEP
    MMA_DRAIN(); T_STAMP(4); T_STAMP(5);
    const int partner = b ^ 4;
#pragma unroll
    for (int u = 0; u < 4; ++u) st_wt(ws, (unsigned)WS_PX + partner * 16384 + (wid * 4 + u) * 1024 + lane * 16, p ? pack44(acc[0][u], acc[1][u]) : pack44(acc[0][4 + u], acc[1][4 + u]));
    publish(ws, 3, 1, partner, 0); T_STAMP(6);
    await<GLD_ACQ>(ws, 3, 1, b); T_STAMP(7);
    const char* gx = ws + WS_PX + b * 16384 + wid * 4096;
    gld1<0>(gx, vf, f0[0]); gld1<1024>(gx, vf, f0[1]); gld1<2048>(gx, vf, f0[2]); gld1<3072>(gx, vf, f0[3]);
    asm volatile("s_waitcnt vmcnt(0)" : "+v"(f0[0]), "+v"(f0[1]), "+v"(f0[2]), "+v"(f0[3]) :: "memory");
    char* dst = out + (size_t)(tb * 128 + p * 64 + rho) * 4096 + eb * 64 + g * 16;
#pragma unroll
    for (int u = 0; u < 4; ++u) {
        const u32x4 mine = p ? pack44(acc[0][4 + u], acc[1][4 + u]) : pack44(acc[0][u], acc[1][u]), other = __builtin_bit_cast(u32x4, f0[u]);
        const u32x4 re = p ? other : mine, im = p ? mine : other;
        *(u32x4*)(dst + (size_t)u * 16 * 4096) = (u32x4){sub_pk(re[0], im[0]), sub_pk(re[1], im[1]), sub_pk(re[2], im[2]), sub_pk(re[3], im[3])};
    }
}

__device__ __forceinline__ float xhalf_max(float m) { auto rr = __builtin_amdgcn_permlane32_swap(__float_as_uint(m), __float_as_uint(m), false, false);
    return __builtin_fmaxf(__uint_as_float(rr[0]), __uint_as_float(rr[1])); }
__device__ __forceinline__ float xhalf_sum(float m) { auto rr = __builtin_amdgcn_permlane32_swap(__float_as_uint(m), __float_as_uint(m), false, false);
    return __uint_as_float(rr[0]) + __uint_as_float(rr[1]); }
__device__ __forceinline__ float rowmax16(const f32x16& s) {
    float a = __builtin_fmaxf(s[0], s[1]);
#pragma unroll
    for (int r = 2; r < 16; r += 2) a = __builtin_fmaxf(__builtin_fmaxf(a, s[r]), s[r + 1]);
    return xhalf_max(a); }
__device__ __forceinline__ float max16(const f32x16& s, float a) {
#pragma unroll
    for (int r = 0; r < 16; r += 2) a = __builtin_fmaxf(__builtin_fmaxf(a, s[r]), s[r + 1]);
    return a; }
constexpr int REF = 64;
__device__ __forceinline__ unsigned rescale_pk(unsigned w, int d) {
    return cvtpk(__builtin_ldexpf(__uint_as_float(w << 16), d), __builtin_ldexpf(__uint_as_float(w & 0xffff0000u), d)); }

__device__ __forceinline__ void role_att(int b, char* ws, char* lds) {
    const int lane = threadIdx.x & 63, wid = __builtin_amdgcn_readfirstlane(threadIdx.x >> 6);
    const int x = b & 7, i = b >> 3;
    const int hd = x * 4 + (i >> 3), tt0 = (i & 7) * 8 + wid * 2;
    const char* gkv = ws + WS_KV + ((size_t)hd << 18);
    const unsigned vl = lane * 16;
    const unsigned ldw = (unsigned)(uintptr_t)lds + wid * 4096;
#define AT_DMA(st) dma4dev(gkv + (st) * 16384 + wid * 4096, ldw + ((st) & 3) * 16384, vl)
    await<GLD_ACQ>(ws, 0, 16, b); T_STAMP(3);
    AT_DMA(0); AT_DMA(1);
    const unsigned gq = (unsigned)WS_Q + ((hd * 64 + tt0) << 11) + vl;
    const bf16x8 q00 = ld_dev(ws, gq), q01 = ld_dev(ws, gq + 1024), q10 = ld_dev(ws, gq + 2048), q11 = ld_dev(ws, gq + 3072);
    bf16x8 sel0, sel1;
    { const bool on = ((lane >> 2) & 1) == ((lane >> 4) & 1); const short v0 = (on && (lane & 3) == 0) ? (short)0x3F80 : (short)0, v1 = (on && (lane & 3) == 1) ? (short)0x3F80 : (short)0;
      sel0 = (bf16x8){v0, v0, v0, v0, v0, v0, v0, v0}; sel1 = (bf16x8){v1, v1, v1, v1, v1, v1, v1, v1}; PIN(sel0); PIN(sel1); }
    const lds_cptr lp = (lds_cptr)lds + vl;
    f32x16 o0 = {}, o1 = {}, nm0, nm1, sA, sB = {}; f32x4 L = {};
    u32x4 pA0 = {}, pA1 = {}, pB0 = {}, pB1 = {};
    bf16x8 k0, k1, kn0, kn1, v0, v1, vp0 = {}, vp1 = {};
    WAIT_BAR(0);
    k0 = ldsr(lp); k1 = ldsr(lp + 1024);
    {
        const f32x16 z = {}; float m0 = -__builtin_inff(), m1 = m0;
#pragma unroll
        for (int kt = 0; kt < 4; ++kt) { const bf16x8 ka = ldsr(lp + kt * 4096), kb = ldsr(lp + kt * 4096 + 1024);
            m0 = max16(MFMA32(kb, q01, MFMA32(ka, q00, z)), m0); m1 = max16(MFMA32(kb, q11, MFMA32(ka, q10, z)), m1); }
        m0 = xhalf_max(m0) + (float)REF; m1 = xhalf_max(m1) + (float)REF;
#pragma unroll
        for (int r = 0; r < 16; ++r) { nm0[r] = -m0; nm1[r] = -m1; }
        PIN(nm0); PIN(nm1);
    }
    sA = MFMA32(k1, q01, MFMA32(k0, q00, nm0));
#define EX(x) __builtin_amdgcn_exp2f(x)
#define BF(x) __builtin_bit_cast(bf16x8, x)
#define E_(S, i) do { S[i] = EX(S[i]); SBAR(); } while (0)
#define C_(P, j, S, i) do { P[j] = cvtpk(S[i], S[i + 1]); SBAR(); } while (0)
#define BODY(SC, SN, PC0, PC1, PP0, PP1, KA, KB, QA, QB, NM, VA, VB, OP, SEL, LD0, LD1) do { \
        SN = MFMA32(KA, QA, NM); SBAR(); LD0; SBAR(); E_(SC, 0); E_(SC, 1); E_(SC, 2); E_(SC, 3); PIN(SC); SBAR(); \
        OP = MFMA32(VA, BF(PP0), OP); SBAR(); LD1; SBAR(); E_(SC, 4); E_(SC, 5); E_(SC, 6); E_(SC, 7); PIN(SC); SBAR(); \
        SN = MFMA32(KB, QB, SN); SBAR(); E_(SC, 8); E_(SC, 9); E_(SC, 10); E_(SC, 11); PIN(SC); SBAR(); \
        L = MFMA16(SEL, BF(PP0), L); SBAR(); E_(SC, 12); E_(SC, 13); PIN(SC); SBAR(); \
        L = MFMA16(SEL, BF(PP1), L); SBAR(); E_(SC, 14); E_(SC, 15); PIN(SC); SBAR(); \
        OP = MFMA32(VB, BF(PP1), OP); SBAR(); C_(PC0, 0, SC, 0); C_(PC0, 1, SC, 2); C_(PC0, 2, SC, 4); C_(PC0, 3, SC, 6); C_(PC1, 0, SC, 8); C_(PC1, 1, SC, 10); C_(PC1, 2, SC, 12); C_(PC1, 3, SC, 14); PIN(PC0); PIN(PC1); SBAR(); \
    } while (0)
#define KTILE(KO, KN) do { \
        BODY(sA, sB, pA0, pA1, pB0, pB1, k0, k1, q10, q11, nm1, vp0, vp1, o1, sel1, (kn0 = ldsr(lp + (KN)), kn1 = ldsr(lp + (KN) + 1024)), (v0 = ldsr(lp + (KO) + 2048), v1 = ldsr(lp + (KO) + 3072))); \
        BODY(sB, sA, pB0, pB1, pA0, pA1, kn0, kn1, q00, q01, nm0, v0, v1, o0, sel0, (void)0, (void)0); \
        k0 = kn0; k1 = kn1; vp0 = v0; vp1 = v1; } while (0)
    T_STAMP(4);
    for (int st = 0; st < 16; ++st) {
        if (st) WAIT_BAR(0);
        if (st + 2 < 16) AT_DMA(st + 2);
        if (__builtin_expect(st == 4 || __any(__builtin_fmaxf(L[0], L[1]) > 0x1p40f), 0)) {
            const int d0 = __builtin_amdgcn_frexp_expf(L[0]) + REF, d1 = __builtin_amdgcn_frexp_expf(L[1]) + REF;
            const float f0 = (float)d0, f1 = (float)d1;
            L[0] = __builtin_ldexpf(L[0], -d0); L[1] = __builtin_ldexpf(L[1], -d1);
#pragma unroll
            for (int r = 0; r < 16; ++r) { o0[r] = __builtin_ldexpf(o0[r], -d0); o1[r] = __builtin_ldexpf(o1[r], -d1); nm0[r] -= f0; nm1[r] -= f1; sA[r] -= f0; }
#pragma unroll
            for (int r = 0; r < 4; ++r) { pB0[r] = rescale_pk(pB0[r], -d1); pB1[r] = rescale_pk(pB1[r], -d1); }
        }
        const int so = (st & 3) * 16384, sn = ((st + 1) & 3) * 16384;
        KTILE(so, so + 4096); KTILE(so + 4096, so + 8192); KTILE(so + 8192, so + 12288); KTILE(so + 12288, sn);
    }
    o1 = MFMA32(vp0, BF(pB0), o1); L = MFMA16(sel1, BF(pB0), L); o1 = MFMA32(vp1, BF(pB1), o1); L = MFMA16(sel1, BF(pB1), L);
    T_STAMP(5);
    float l0 = L[0], l1 = L[1];
    {
        float t0 = 0.f, t1 = 0.f;
#pragma unroll
        for (int r = 0; r < 16; ++r) { t0 += __builtin_fabsf(o0[r]); t1 += __builtin_fabsf(o1[r]); }
        if (__builtin_expect(__any(!(t0 + t1 + l0 + l1 < __builtin_inff()) || !(l0 > 0.f) || !(l1 > 0.f) || !(t0 > 0x1p-100f) || !(t1 > 0x1p-100f)), 0)) {
            float m0 = -__builtin_inff(), m1 = m0; l0 = 0.f; l1 = 0.f; o0 = f32x16{}; o1 = f32x16{};
            for (int kt = 0; kt < 64; ++kt) {
                const unsigned g = (unsigned)WS_KV + (hd << 18) + kt * 4096 + vl;
                const bf16x8 ka = ld_dev(ws, g), kb = ld_dev(ws, g + 1024), va = ld_dev(ws, g + 2048), vb = ld_dev(ws, g + 3072);
                f32x16 z = {};
                f32x16 s0 = MFMA32(kb, q01, MFMA32(ka, q00, z)), s1 = MFMA32(kb, q11, MFMA32(ka, q10, z));
                const float n0 = __builtin_fmaxf(m0, rowmax16(s0)), n1 = __builtin_fmaxf(m1, rowmax16(s1));
                const float c0 = (n0 == -__builtin_inff()) ? 0.f : n0, c1 = (n1 == -__builtin_inff()) ? 0.f : n1;
                const float a0 = EX(m0 - c0), a1 = EX(m1 - c1); m0 = n0; m1 = n1;
#pragma unroll
                for (int r = 0; r < 16; ++r) { s0[r] = EX(s0[r] - c0); s1[r] = EX(s1[r] - c1); o0[r] *= a0; o1[r] *= a1; }
                const u32x4 a = pack8(s0, 0), bb = pack8(s0, 8), c = pack8(s1, 0), d = pack8(s1, 8);
                f32x4 ls = {}; ls = MFMA16(sel0, BF(a), ls); ls = MFMA16(sel0, BF(bb), ls); ls = MFMA16(sel1, BF(c), ls); ls = MFMA16(sel1, BF(d), ls);
                l0 = l0 * a0 + ls[0]; l1 = l1 * a1 + ls[1];
                o0 = MFMA32(vb, BF(bb), MFMA32(va, BF(a), o0)); o1 = MFMA32(vb, BF(d), MFMA32(va, BF(c), o1));
            }
        }
    }
    const float r0 = 1.0f / l0, r1 = 1.0f / l1;
    const unsigned go = (unsigned)WS_O + ((tt0 * 32 + hd) << 11) + ((lane >> 4) & 1) * 1024 + (lane >> 5) * 256 + (lane & 15) * 16;
    st_wt(ws, go, pack8(o0, 0, r0)); st_wt(ws, go + 512, pack8(o0, 8, r0));
    st_wt(ws, go + 65536, pack8(o1, 0, r1)); st_wt(ws, go + 65536 + 512, pack8(o1, 8, r1));
    publish(ws, 2, 32, g4_index((i & 7) * 2 + ((lane >> 4) & 1), lane & 15), hd);
#undef E_
#undef C_
#undef AT_DMA
#undef BODY
#undef KTILE
}

__device__ __forceinline__ void role_g4(int b, const char* wo, char* ws, char* out, char* lds) {
    const int lane = threadIdx.x & 63, wid = __builtin_amdgcn_readfirstlane(threadIdx.x >> 6);
    const int x = b & 7, i = b >> 3;
    const int tb = (x >> 1) * 4 + (i & 3), nb = (x & 1) * 8 + (i >> 2);
    const char* gw = wo + nb * 128;
    const char* gf = ws + WS_O + ((size_t)(tb * 4 + wid) << 16);
    unsigned vw[2];
#pragma unroll
    for (int j = 0; j < 2; ++j) vw[j] = nrow_off(wid * 16, j, lane, 2048);
    const unsigned vf = lane * 16;
    const unsigned ldw = (unsigned)(uintptr_t)lds + wid * 2048;
    const int rho = lane & 15, g = lane >> 4, q4 = (lane >> 2) & 3;
    const int k0 = 16 * (g >> 1) + 4 * (g & 1) + q4;
    const int ow = k0 * 128 + (tsw(k0) << 5) + (lane & 3) * 8;
    f32x4 acc[2][4] = {};
#pragma unroll
    for (int n = 0; n < 8; ++n) PIN(acc[n >> 2][n & 3]);
    bf16x8 fo[8][4]; s16x4 wl[2][4], wh[2][4];
    const int kr0 = 4 * (i & 3) + (x >> 1) + 2 * (i >> 2) + (x & 1);
#define G4_W(k) do { const char* w_ = gw + (((k) + kr0) & 15) * (64 * 2048); const unsigned d_ = ldw + ((k) % 10) * 8192; dma1<0>(w_, d_, vw[0]); dma1<1024>(w_, d_, vw[1]); } while (0)
#define G4_ISS(n, k) do { const char* f_ = gf + (((k) + kr0) & 15) * 4096; \
        if ((n) == 0) gld1<0>(f_, vf, fo[(k) & 7][0]); if ((n) == 1) gld1<1024>(f_, vf, fo[(k) & 7][1]); if ((n) == 2) gld1<2048>(f_, vf, fo[(k) & 7][2]); if ((n) == 3) gld1<3072>(f_, vf, fo[(k) & 7][3]); } while (0)
#define G4_HALF(RS, slot, KK, RD, MM, B0, B1, IS, k) do { \
        _Pragma("unroll") for (int n = 0; n < 4; ++n) { \
            if (RD) { const lds_cptr w_ = (lds_cptr)lds + (slot) * 8192 + (KK) * 4096; wl[RS][n] = ldstr(w_ + (ow ^ (n * 32))); wh[RS][n] = ldstr(w_ + (ow ^ (n * 32) ^ 64) + 1024); } \
            if (MM) { const bf16x8 w_ = cat(wl[RS ^ 1][n], wh[RS ^ 1][n]); mma16(acc[0][n], w_, B0); mma16(acc[1][n], w_, B1); } \
            if (IS) G4_ISS(n, k); \
            SBAR(); } } while (0)
#define G4_STEP(W, k) do { __builtin_amdgcn_s_waitcnt(0xC07F); \
        asm volatile("s_waitcnt vmcnt(%4)\n\ts_barrier" : "+v"(fo[(k) & 7][0]), "+v"(fo[(k) & 7][1]), "+v"(fo[(k) & 7][2]), "+v"(fo[(k) & 7][3]) : "i"(W) : "memory"); \
        if ((k) >= 1 && (k) + 9 < 16) G4_W((k) + 9); \
        G4_HALF(0, (k) % 10, 0, true, (k) >= 1, fo[((k) + 7) & 7][2], fo[((k) + 7) & 7][3], false, 0); \
        G4_HALF(1, (k) % 10, 1, true, true, fo[(k) & 7][0], fo[(k) & 7][1], (k) >= 1 && (k) + 7 < 16, (k) + 7); } while (0)
#pragma unroll
    for (int k = 0; k < 10; ++k) G4_W(k);
    await<GLD_ACQ>(ws, 2, 32, b); T_STAMP(7);
#pragma unroll
    for (int n = 0; n < 32; ++n) G4_ISS(n & 3, n >> 2);
    G4_STEP(28, 0); G4_STEP(24, 1); G4_STEP(26, 2); G4_STEP(28, 3); G4_STEP(30, 4); G4_STEP(32, 5); G4_STEP(34, 6); G4_STEP(36, 7);
    G4_STEP(34, 8); G4_STEP(32, 9); G4_STEP(26, 10); G4_STEP(20, 11); G4_STEP(14, 12); G4_STEP(8, 13); G4_STEP(4, 14); G4_STEP(0, 15);
    G4_HALF(0, 0, 0, false, true, fo[7][2], fo[7][3], false, 0);
#undef G4_W
#undef G4_ISS
#undef G4_HALF
#undef G4_STEP
    MMA_DRAIN();
    char* d = out + (size_t)(tb * 128 + wid * 32 + rho) * 4096 + 2048 + nb * 128 + g * 8;
#pragma unroll
    for (int u = 0; u < 2; ++u)
#pragma unroll
        for (int n = 0; n < 4; ++n) *(uint2*)(d + u * 16 * 4096 + n * 32) = make_uint2(cvtpk(acc[u][n][0], acc[u][n][1]), cvtpk(acc[u][n][2], acc[u][n][3]));
}

#define PRIO_B 3
__global__ __launch_bounds__(256, 2) void k_fused(const char* xA, const char* wsre, const char* wsim, const char* wre, const char* wim,
                                                   const char* xB, const char* wqkv, const char* wo, char* ws, char* out) {
    extern __shared__ __attribute__((aligned(16))) char lds[];
    T_STAMP(0);
    if (blockIdx.x < 256) {
        __builtin_amdgcn_s_setprio(PRIO_B);
        T_STAMP(9); role_g2(blockIdx.x, xB, wqkv, ws, lds); T_STAMP(2);
        role_att(blockIdx.x, ws, lds); T_STAMP(6);
        role_g4(blockIdx.x, wo, ws, out, lds); T_STAMP(8);
    } else {
        role_g1(blockIdx.x - 256, xA, wre, wim, ws, lds); T_STAMP(2);
        role_g3(blockIdx.x - 256, wsre, wsim, wo, ws, out, lds); T_STAMP(8);
    }
}

extern "C" void kernel_launch(void* const* d_in, const int* in_sizes, int n_in, void* d_out, int out_size, void* d_ws, size_t ws_size, hipStream_t stream) {
    (void)hipFuncSetAttribute((const void*)k_fused, hipFuncAttributeMaxDynamicSharedMemorySize, LDS_BYTES);
    k_fused<<<512, 256, LDS_BYTES, stream>>>((const char*)d_in[0], (const char*)d_in[1], (const char*)d_in[2], (const char*)d_in[3], (const char*)d_in[4],
                                             (const char*)d_in[5], (const char*)d_in[6], (const char*)d_in[7], (char*)d_ws, (char*)d_out);
}
```

```cpp
#include <hip/hip_runtime.h>
#include <hip/hip_bf16.h>

typedef short bf16x8 __attribute__((ext_vector_type(8)));
typedef short s16x4 __attribute__((ext_vector_type(4)));
typedef float f32x16 __attribute__((ext_vector_type(16)));
typedef float f32x4 __attribute__((ext_vector_type(4)));
typedef unsigned u32x4 __attribute__((ext_vector_type(4)));
#define LAS __attribute__((address_space(3)))
typedef LAS const char* lds_cptr;

#define MFMA32(a, b, c) __builtin_amdgcn_mfma_f32_32x32x16_bf16(a, b, c, 0, 0, 0)
#define MFMA16(a, b, c) __builtin_amdgcn_mfma_f32_16x16x32_bf16(a, b, c, 0, 0, 0)
#define SBAR() __builtin_amdgcn_sched_barrier(0)
#define PIN(x) asm volatile("" : "+v"(x))

constexpr int S_ = 2048, D_ = 1024;
constexpr int LDS_BYTES = 81920;
constexpr size_t WS_AB = 0;
constexpr size_t WS_Q = WS_AB + (8u << 20);
constexpr size_t WS_KV = WS_Q + (4u << 20);
constexpr size_t WS_O = WS_KV + (8u << 20);
constexpr size_t WS_PX = WS_O + (4u << 20);
constexpr size_t WS_FLAG = WS_PX + (4u << 20);
constexpr unsigned long long MAGIC = 0x9E3779B97F4A7C15ull;
typedef __attribute__((address_space(1))) unsigned long long gu64;

typedef float f32x2 __attribute__((ext_vector_type(2)));
typedef __bf16 bf16x2_t __attribute__((ext_vector_type(2)));
__device__ __forceinline__ unsigned cvtpk(float lo, float hi) { return __builtin_bit_cast(unsigned, __builtin_convertvector((f32x2){lo, hi}, bf16x2_t)); }
__device__ __forceinline__ u32x4 pack8(const f32x16& a, int o, float sc) {
    return (u32x4){cvtpk(a[o] * sc, a[o + 1] * sc), cvtpk(a[o + 2] * sc, a[o + 3] * sc), cvtpk(a[o + 4] * sc, a[o + 5] * sc), cvtpk(a[o + 6] * sc, a[o + 7] * sc)}; }
__device__ __forceinline__ u32x4 pack8(const f32x16& a, int o) {
    return (u32x4){cvtpk(a[o], a[o + 1]), cvtpk(a[o + 2], a[o + 3]), cvtpk(a[o + 4], a[o + 5]), cvtpk(a[o + 6], a[o + 7])}; }
__device__ __forceinline__ bf16x8 ldsr(lds_cptr p) { return *(const LAS bf16x8*)p; }
typedef short v4i16_t __attribute__((ext_vector_type(4)));
__device__ __forceinline__ s16x4 ldstr(lds_cptr p) { return __builtin_bit_cast(s16x4, __builtin_amdgcn_ds_read_tr16_b64_v4i16((LAS v4i16_t*)p)); }
__device__ __forceinline__ bf16x8 cat(s16x4 a, s16x4 b) { return (bf16x8){a[0], a[1], a[2], a[3], b[0], b[1], b[2], b[3]}; }

#define T_STAMP(k)
__device__ __forceinline__ u32x4 pack44(const f32x4& a, const f32x4& b) { return (u32x4){cvtpk(a[0], a[1]), cvtpk(a[2], a[3]), cvtpk(b[0], b[1]), cvtpk(b[2], b[3])}; }
__device__ __forceinline__ u32x4 pack44(const f32x4& a, const f32x4& b, float s) { return pack44(a * s, b * s); }

template <int OFF> __device__ __forceinline__ void dma1(const char* sbase, unsigned lds, unsigned v) {
    asm volatile("s_mov_b32 m0, %1\n\ts_nop 0\n\tglobal_load_lds_dwordx4 %2, %0 offset:%3" :: "s"(sbase), "s"(lds), "v"(v), "i"(OFF) : "memory"); }
template <int OFF> __device__ __forceinline__ void dma1nt(const char* sbase, unsigned lds, unsigned v) {
    asm volatile("s_mov_b32 m0, %1\n\ts_nop 0\n\tglobal_load_lds_dwordx4 %2, %0 offset:%3 nt" :: "s"(sbase), "s"(lds), "v"(v), "i"(OFF) : "memory"); }
__device__ __forceinline__ void dma4(const char* sbase, unsigned lds, unsigned v0, unsigned v1, unsigned v2, unsigned v3) {
    asm volatile("s_mov_b32 m0, %1\n\ts_nop 0\n\t"
                 "global_load_lds_dwordx4 %2, %0\n\t"
                 "global_load_lds_dwordx4 %3, %0 offset:1024\n\t"
                 "global_load_lds_dwordx4 %4, %0 offset:2048\n\t"
                 "global_load_lds_dwordx4 %5, %0 offset:3072"
                 :: "s"(sbase), "s"(lds), "v"(v0), "v"(v1), "v"(v2), "v"(v3) : "memory"); }
#define GLD_SC " sc1"
#define GLD_ACQ false
__device__ __forceinline__ void dma4dev(const char* sbase, unsigned lds, unsigned v) {
    asm volatile("s_mov_b32 m0, %1\n\ts_nop 0\n\t"
                 "global_load_lds_dwordx4 %2, %0" GLD_SC "\n\t"
                 "global_load_lds_dwordx4 %2, %0 offset:1024" GLD_SC "\n\t"
                 "global_load_lds_dwordx4 %2, %0 offset:2048" GLD_SC "\n\t"
                 "global_load_lds_dwordx4 %2, %0 offset:3072" GLD_SC
                 :: "s"(sbase), "s"(lds), "v"(v) : "memory"); }
__device__ __forceinline__ bf16x8 ld_dev(char* ws, unsigned off) {
    return __builtin_bit_cast(bf16x8, __builtin_amdgcn_raw_buffer_load_b128(__builtin_amdgcn_make_buffer_rsrc(ws, 0, 0x7fffffff, 0x00020000), off, 0, 16)); }
template <int OFF> __device__ __forceinline__ void gld1(const char* sbase, unsigned voff, bf16x8& a) {
    asm volatile("global_load_dwordx4 %0, %2, %1 offset:%3" GLD_SC : "=&v"(a) : "s"(sbase), "v"(voff), "i"(OFF) : "memory"); }
__device__ __forceinline__ void touch(const char* sbase, unsigned voff, unsigned& sink) {
    asm volatile("global_load_dword %0, %2, %1" : "+v"(sink) : "s"(sbase), "v"(voff) : "memory"); }
#define WAIT_BAR(N) do { __builtin_amdgcn_s_waitcnt(0xC07F); asm volatile("s_waitcnt vmcnt(" #N ")\n\ts_barrier" ::: "memory"); } while (0)
#define WAIT_BAR4(N, R) do { __builtin_amdgcn_s_waitcnt(0xC07F); asm volatile("s_waitcnt vmcnt(" #N ")\n\ts_barrier" : "+v"(R[0]), "+v"(R[1]), "+v"(R[2]), "+v"(R[3]) :: "memory"); } while (0)

__device__ __forceinline__ void mma16(f32x4& c, const bf16x8& a, const bf16x8& b) { asm volatile("v_mfma_f32_16x16x32_bf16 %0, %1, %2, %0" : "+v"(c) : "v"(a), "v"(b)); }
#define MMA_DRAIN() do { SBAR(); asm volatile("s_nop 7\n\ts_nop 7"); SBAR(); } while (0)
__device__ __forceinline__ void st_wt(char* ws, unsigned off, u32x4 v) {
    __builtin_amdgcn_raw_buffer_store_b128(v, __builtin_amdgcn_make_buffer_rsrc(ws, 0, 0x7fffffff, 0x00020000), off, 0, 16); }
#define OUT_AUX 0
__device__ __forceinline__ void st_out16(char* out, unsigned off, u32x4 v) {
    __builtin_amdgcn_raw_buffer_store_b128(v, __builtin_amdgcn_make_buffer_rsrc(out, 0, 8u << 20, 0x00020000), off, 0, OUT_AUX);
}
typedef unsigned u32x2 __attribute__((ext_vector_type(2)));
__device__ __forceinline__ void st_out8(char* out, unsigned off, u32x2 v) {
    __builtin_amdgcn_raw_buffer_store_b64(v, __builtin_amdgcn_make_buffer_rsrc(out, 0, 8u << 20, 0x00020000), off, 0, OUT_AUX);
}
__device__ __forceinline__ gu64* flag_at(char* ws, int kind, int consumer, int slot) { return (gu64*)(ws + WS_FLAG) + ((kind * 256 + consumer) * 32 + slot); }
__device__ __forceinline__ void publish(char* ws, int kind, int n, int consumer, int slot) {
    asm volatile("s_waitcnt vmcnt(0) lgkmcnt(0)\n\ts_barrier" ::: "memory");
    if (threadIdx.x < n) __hip_atomic_store(flag_at(ws, kind, consumer, slot), MAGIC, __ATOMIC_RELAXED, __HIP_MEMORY_SCOPE_AGENT);
}
#define POLL_NAP 4
template <bool ACQ, int NAP = POLL_NAP> __device__ __forceinline__ void await(char* ws, int kind, int n, int me, int slot0 = 0) {
    if (__builtin_amdgcn_readfirstlane(threadIdx.x) == 0) {
        gu64* f = flag_at(ws, kind, me, slot0 + (threadIdx.x & (n - 1)));
        const unsigned long long t0 = __builtin_amdgcn_s_memrealtime();
        while (!__all(__hip_atomic_load(f, __ATOMIC_RELAXED, __HIP_MEMORY_SCOPE_AGENT) == MAGIC)) {
            if (__builtin_amdgcn_s_memrealtime() - t0 > 20000000ull) break;
            __builtin_amdgcn_s_sleep(NAP); }
        if (threadIdx.x < n) __hip_atomic_store(f, 0ull, __ATOMIC_RELAXED, __HIP_MEMORY_SCOPE_AGENT);
        if (ACQ) { __builtin_amdgcn_fence(__ATOMIC_ACQUIRE, "agent"); asm volatile("s_waitcnt vmcnt(0)" ::: "memory"); }
    }
    asm volatile("s_barrier" ::: "memory");
}

__device__ __forceinline__ int fsw(int r) { return ((r >> 1) & 7) ^ (((r >> 4) & 1) * 6); }
__device__ __forceinline__ int tsw(int k) { return ((k >> 1) & 1) | ((((k >> 2) ^ (k >> 3)) & 1) << 1); }
__device__ __forceinline__ int perm32(int rho, int T) { return 8 * (rho >> 2) + 4 * T + (rho & 3); }
__device__ __forceinline__ int tokmap(int rho, int T) { return 16 * (rho >> 3) + 8 * T + (rho & 7); }
__device__ __forceinline__ unsigned krow_off(int row0, int j, int lane, int ld) {
    const int r = row0 + 8 * j + (lane >> 3);
    return (unsigned)(r * ld + (((lane & 7) ^ fsw(r)) << 4) - 1024 * j); }
__device__ __forceinline__ unsigned nrow_off(int k0, int j, int lane, int ld) {
    const int k = k0 + 8 * j + (lane >> 3);
    return (unsigned)(k * ld + (((lane & 7) ^ (tsw(k) << 1)) << 4) - 1024 * j); }
__device__ __forceinline__ int frag_off(int row, int g) { return row * 128 + ((g ^ fsw(row)) << 4); }

__device__ __forceinline__ int krot(int x, int i) { return 2 * (i & 7) + (x >> 2) + 4 * (i >> 3) + (x & 3); }
#define RB 4
__device__ __forceinline__ int krot0(int x, int i) { return RB == 16 ? krot(x, i) : RB == 8 ? (i & 7) + 2 * (i >> 3) : (i & 7) + (i >> 3); }
__device__ __forceinline__ int kidx(int k, int k0) { return (k & ~(RB - 1)) | ((k + k0) & (RB - 1)); }
__device__ __forceinline__ int g3_index(int p, int tb, int et) { return (p * 4 + (tb >> 2)) + 8 * ((tb & 3) + 4 * et); }
__device__ __forceinline__ int att_index(int hd, int qb) { return (hd >> 2) + 8 * ((hd & 3) * 8 + qb); }
__device__ __forceinline__ int g4_index(int tb, int nb) { return ((tb >> 2) * 2 + (nb >> 3)) + 8 * ((tb & 3) + 4 * (nb & 7)); }

__device__ __forceinline__ void role_g1(int b, const char* xA, const char* wre, const char* wim, char* ws, char* lds) {
    const int lane = threadIdx.x & 63, wid = __builtin_amdgcn_readfirstlane(threadIdx.x >> 6);
    const int x = b & 7, i = b >> 3;
    const int sm = (x >> 2) * 8 + (i & 7), en = (x & 3) * 4 + (i >> 3);
    const int p = en >> 3, e0 = (en & 7) * 128, s0 = sm * 128;
    const char* ga = xA + (size_t)s0 * 2048;
    const char* gb = (p ? wim : wre) + (size_t)e0 * 2048;
    unsigned vo[4];
#pragma unroll
    for (int j = 0; j < 4; ++j) vo[j] = krow_off(wid * 32, j, lane, 2048);
    const unsigned ldw = (unsigned)(uintptr_t)lds + wid * 4096;
    const int wm = wid >> 1, wn = wid & 1, rho = lane & 15, g = lane >> 4;
    int oa[2], ob[2];
#pragma unroll
    for (int T = 0; T < 2; ++T) { oa[T] = frag_off(wm * 64 + perm32(rho, T), g); ob[T] = 16384 + frag_off(wn * 64 + perm32(rho, T), g); }
    f32x4 acc[4][4] = {};
#pragma unroll
    for (int n = 0; n < 16; ++n) PIN(acc[n >> 2][n & 3]);
    bf16x8 fa[3][4], fb[3][4];
#define A_NAP 100
    __builtin_amdgcn_s_sleep(A_NAP);
    T_STAMP(9);
    const int k0 = krot0(x, i);
#define G1_DMA(n, kn, CUR) do { const unsigned d_ = ldw + (CUR); const int ko_ = kidx(kn, k0) * 128; \
        if ((n) == 0) dma1<0>(ga + ko_, d_, vo[0]); if ((n) == 1) dma1<0>(gb + ko_, d_ + 16384, vo[0]); \
        if ((n) == 2) dma1<1024>(ga + ko_, d_, vo[1]); if ((n) == 3) dma1<1024>(gb + ko_, d_ + 16384, vo[1]); \
        if ((n) == 4) dma1<2048>(ga + ko_, d_, vo[2]); if ((n) == 5) dma1<2048>(gb + ko_, d_ + 16384, vo[2]); \
        if ((n) == 6) dma1<3072>(ga + ko_, d_, vo[3]); if ((n) == 7) dma1<3072>(gb + ko_, d_ + 16384, vo[3]); } while (0)
#define G1_RD(RS, CUR, KK, n) do { if ((n) < 4) fa[RS][(n) & 3] = ldsr((lds_cptr)lds + (CUR) + (oa[(n) & 1] ^ ((KK) * 64)) + (((n) >> 1) & 1) * 4096); \
                                   else fb[RS][(n) & 3] = ldsr((lds_cptr)lds + (CUR) + (ob[(n) & 1] ^ ((KK) * 64)) + (((n) >> 1) & 1) * 4096); } while (0)
#define G1_MM(S, n) do { mma16(acc[(n) >> 1][((n) & 1) * 2], fa[S][(n) >> 1], fb[S][((n) & 1) * 2]); mma16(acc[(n) >> 1][((n) & 1) * 2 + 1], fa[S][(n) >> 1], fb[S][((n) & 1) * 2 + 1]); } while (0)
#define G1_STEP(W, CUR, SB, SC, MM, DM, kn) do { WAIT_BAR(W); \
        _Pragma("unroll") for (int n = 0; n < 8; ++n) { G1_RD(0, CUR, 0, n); G1_RD(SB, CUR, 1, n); if (MM) G1_MM(SC, n); SBAR(); } \
        if (DM) WAIT_BAR(63); \
        _Pragma("unroll") for (int n = 0; n < 8; ++n) { G1_MM(0, n); if (DM) G1_DMA(n, kn, CUR); SBAR(); } } while (0)
#pragma unroll
    for (int n = 0; n < 16; ++n) G1_DMA(n & 7, n >> 3, (n >> 3) * 32768);
    G1_STEP(8, 0, 1, 2, false, true, 2);
    for (int kt = 1; kt < 13; kt += 2) { G1_STEP(8, 32768, 2, 1, true, true, kt + 2); G1_STEP(8, 0, 1, 2, true, true, kt + 3); }
    G1_STEP(8, 32768, 2, 1, true, true, 15); G1_STEP(8, 0, 1, 2, true, false, 0); G1_STEP(0, 32768, 2, 1, true, false, 0);
#pragma unroll
    for (int n = 0; n < 8; ++n) G1_MM(2, n);
#undef G1_DMA
#undef G1_MM
#undef G1_STEP
#undef G1_RD
    MMA_DRAIN(); T_STAMP(1);
#pragma unroll
    for (int blk = 0; blk < 2; ++blk)
#pragma unroll
        for (int n = 0; n < 4; ++n) {
            const int eb = (e0 >> 5) + wn * 2 + (n >> 1), sb = (s0 >> 5) + wm * 2 + blk;
            st_wt(ws, (unsigned)WS_AB + ((((p * 32 + eb) * 64 + sb) * 2 + (n & 1)) << 10) + lane * 16, pack44(acc[2 * blk][n], acc[2 * blk + 1][n]));
        }
    publish(ws, 1, 16, g3_index(p, lane & 15, en & 7), sm);
}

__device__ __forceinline__ void role_g2(int b, const char* xB, const char* wqkv, char* ws, char* lds) {
    const int lane = threadIdx.x & 63, wid = __builtin_amdgcn_readfirstlane(threadIdx.x >> 6);
    const int x = b & 7, i = b >> 3;
    const int tm = (x >> 2) * 8 + (i & 7), hp = (x & 3) * 4 + (i >> 3);
    const char* ga = xB + (size_t)tm * 128 * 2048;
    const char* gw = wqkv + hp * 128;
    unsigned vo[4], vw[2];
#pragma unroll
    for (int j = 0; j < 4; ++j) vo[j] = krow_off(wid * 32, j, lane, 2048);
#pragma unroll
    for (int j = 0; j < 2; ++j) vw[j] = nrow_off(wid * 16, j, lane, 6144);
    const unsigned lds0 = (unsigned)(uintptr_t)lds;
    const int wm = wid >> 1, wn = wid & 1, rho = lane & 15, g = lane >> 4, q4 = (lane >> 2) & 3;
    int oa[2];
#pragma unroll
    for (int T = 0; T < 2; ++T) oa[T] = frag_off(wm * 64 + tokmap(rho, T), g);
    const int ow = 16384 + (8 * g + q4) * 128 + (((2 * wn) ^ tsw(8 * g + q4)) << 5) + (lane & 3) * 8;
    f32x4 acc[4][6] = {};
#pragma unroll
    for (int n = 0; n < 24; ++n) PIN(acc[n / 6][n % 6]);
    bf16x8 fa[3][4]; s16x4 wl[3][6], wh[3][6];
    const int k0 = krot0(x, i);
#define G2_DMA(n, kt, buf) do { const unsigned d_ = lds0 + (buf); const int kr_ = kidx(kt, k0); const char* w_ = gw + kr_ * (64 * 6144); const char* a_ = ga + kr_ * 128; \
        if ((n) == 0) dma1<0>(a_, d_ + wid * 4096, vo[0]); if ((n) == 1) dma1<0>(w_, d_ + 16384 + wid * 2048, vw[0]); if ((n) == 2) dma1<1024>(w_, d_ + 16384 + wid * 2048, vw[1]); \
        if ((n) == 3) dma1<1024>(a_, d_ + wid * 4096, vo[1]); if ((n) == 4) dma1<0>(w_ + 2048, d_ + 24576 + wid * 2048, vw[0]); if ((n) == 5) dma1<1024>(w_ + 2048, d_ + 24576 + wid * 2048, vw[1]); \
        if ((n) == 6) dma1<2048>(a_, d_ + wid * 4096, vo[2]); if ((n) == 7) dma1<0>(w_ + 4096, d_ + 32768 + wid * 2048, vw[0]); if ((n) == 8) dma1<1024>(w_ + 4096, d_ + 32768 + wid * 2048, vw[1]); \
        if ((n) == 9) dma1<3072>(a_, d_ + wid * 4096, vo[3]); } while (0)
#define G2_RDA(RS, CUR, KK, m) fa[RS][m] = ldsr((lds_cptr)lds + (CUR) + (oa[(m) & 1] ^ ((KK) * 64)) + ((m) >> 1) * 4096)
#define G2_RDW(RS, CUR, KK, c) do { const lds_cptr w_ = (lds_cptr)lds + (CUR) + ((c) >> 1) * 8192 + (KK) * 4096; \
        wl[RS][c] = ldstr(w_ + (ow ^ (((c) & 1) * 32))); wh[RS][c] = ldstr(w_ + (ow ^ (((c) & 1) * 32) ^ 64) + 512); } while (0)
#define G2_MM(S, m, c) do { const bf16x8 w_ = cat(wl[S][c], wh[S][c]); if ((c) < 4) mma16(acc[m][c], w_, fa[S][m]); else mma16(acc[m][c], fa[S][m], w_); } while (0)
#define G2_STEP(W, CUR, SB, SC, MM, DM, kn) do { WAIT_BAR(W); \
        _Pragma("unroll") for (int n = 0; n < 12; ++n) { \
            if (n < 6) G2_RDW(0, CUR, 0, (n < 6 ? n : 0)); else G2_RDW(SB, CUR, 1, (n < 6 ? 0 : n - 6)); \
            if (n < 4) G2_RDA(0, CUR, 0, (n & 3)); else if (n < 8) G2_RDA(SB, CUR, 1, (n & 3)); \
            if (MM) { G2_MM(SC, (n & 1) * 2, n >> 1); G2_MM(SC, (n & 1) * 2 + 1, n >> 1); } \
            SBAR(); } \
        if (DM) WAIT_BAR(63); \
        _Pragma("unroll") for (int n = 0; n < 12; ++n) { G2_MM(0, (n & 1) * 2, n >> 1); G2_MM(0, (n & 1) * 2 + 1, n >> 1); if (DM) G2_DMA(n, kn, CUR); SBAR(); } } while (0)
#pragma unroll
    for (int n = 0; n < 20; ++n) G2_DMA(n % 10, n / 10, (n / 10) * 40960);
    G2_STEP(10, 0, 1, 2, false, true, 2);
    for (int kt = 1; kt < 13; kt += 2) { G2_STEP(10, 40960, 2, 1, true, true, kt + 2); G2_STEP(10, 0, 1, 2, true, true, kt + 3); }
    G2_STEP(10, 40960, 2, 1, true, true, 15); G2_STEP(10, 0, 1, 2, true, false, 0); G2_STEP(0, 40960, 2, 1, true, false, 0);
#pragma unroll
    for (int n = 0; n < 12; ++n) { G2_MM(2, (n & 1) * 2, n >> 1); G2_MM(2, (n & 1) * 2 + 1, n >> 1); }
#undef G2_DMA
#undef G2_RDA
#undef G2_RDW
#undef G2_MM
#undef G2_STEP
    MMA_DRAIN(); T_STAMP(1);
    const int hd = hp * 2 + wn;
    constexpr float QS = 0.17677669529663687f * 1.4426950408889634f;
    const int co = (g >> 1) * 1024 + (g & 1) * 512;
#pragma unroll
    for (int blk = 0; blk < 2; ++blk) {
        const int tt = tm * 4 + wm * 2 + blk;
        const unsigned q = (unsigned)WS_Q + ((hd * 64 + tt) << 11) + co, kv = (unsigned)WS_KV + ((hd * 64 + tt) << 12) + co;
#pragma unroll
        for (int T = 0; T < 2; ++T) {
            const int to = tokmap(rho, T) * 16;
            st_wt(ws, kv + to, pack44(acc[2 * blk + T][2], acc[2 * blk + T][3]));
            st_wt(ws, kv + 2048 + T * 256 + rho * 16, pack44(acc[2 * blk][4 + T], acc[2 * blk + 1][4 + T]));
            st_wt(ws, q + to, pack44(acc[2 * blk + T][0], acc[2 * blk + T][1], QS));
        }
    }
    publish(ws, 0, 16, att_index(hp * 2 + ((lane >> 3) & 1), lane & 7), tm);
}

__device__ __forceinline__ unsigned sub_pk(unsigned a, unsigned b) {
    return cvtpk(__uint_as_float(a << 16) - __uint_as_float(b << 16), __uint_as_float(a & 0xffff0000u) - __uint_as_float(b & 0xffff0000u)); }
#define G3DMA dma1
__device__ __forceinline__ void role_g3(int b, const char* wsre, const char* wsim, const char* wo, char* ws, char* out, char* lds) {
    const int lane = threadIdx.x & 63, wid = __builtin_amdgcn_readfirstlane(threadIdx.x >> 6);
    const int x = b & 7, i = b >> 3;
    const int p = x >> 2, tb = (x & 3) * 4 + (i & 3), eb = (i >> 2) * 4 + wid;
    const char* gt = (p ? wsim : wsre) + (size_t)tb * 128 * 4096;
    const char* gf = ws + WS_AB + ((size_t)(p * 32 + eb) << 17);
    unsigned vo[4];
#pragma unroll
    for (int j = 0; j < 4; ++j) vo[j] = krow_off(wid * 32, j, lane, 4096);
    const unsigned vf = lane * 16;
    const unsigned ldw = (unsigned)(uintptr_t)lds + wid * 4096;
    const int rho = lane & 15, g = lane >> 4;
    int ob[2];
#pragma unroll
    for (int T = 0; T < 2; ++T) ob[T] = frag_off(16 * T + rho, g);
    f32x4 acc[2][8] = {};
#pragma unroll
    for (int n = 0; n < 16; ++n) PIN(acc[n >> 3][n & 7]);
    bf16x8 f0[4], f1[4], f2[4], f3[4], ft[2][8];
    const int kr0 = (i >> 2) + 2 * (i & 3);
#define G3_ISS(n, k, slot, F) do { const int kr_ = ((k) & ~7) | (((k) + kr0) & 7); const char* t_ = gt + kr_ * 128; const char* f_ = gf + kr_ * 4096; const unsigned d_ = ldw + (slot) * 16384; \
        if ((n) == 0) G3DMA<0>(t_, d_, vo[0]); if ((n) == 1) G3DMA<1024>(t_, d_, vo[1]); if ((n) == 2) G3DMA<2048>(t_, d_, vo[2]); if ((n) == 3) G3DMA<3072>(t_, d_, vo[3]); \
        if ((n) == 4) gld1<0>(f_, vf, F[0]); if ((n) == 5) gld1<1024>(f_, vf, F[1]); if ((n) == 6) gld1<2048>(f_, vf, F[2]); if ((n) == 7) gld1<3072>(f_, vf, F[3]); } while (0)
#define G3_HALF(RS, slot, KK, RD, MM, A0, A1, IS, I0, k, islot, F) do { \
        _Pragma("unroll") for (int n = 0; n < 8; ++n) { \
            if (RD) ft[RS][n] = ldsr((lds_cptr)lds + (slot) * 16384 + (ob[n & 1] ^ ((KK) * 64)) + (n >> 1) * 4096); \
            if (MM) { mma16(acc[0][n], A0, ft[RS ^ 1][n]); mma16(acc[1][n], A1, ft[RS ^ 1][n]); } \
            if ((IS) && (n & 1) == 0) G3_ISS((I0) + (n >> 1), k, islot, F); \
            SBAR(); } } while (0)
#define G3_STEP(W, slot, FC, FP, MM, IS, k, islot) do { WAIT_BAR4(W, FC); \
        G3_HALF(0, slot, 0, true, MM, FP[2], FP[3], IS, 0, k, islot, FP); G3_HALF(1, slot, 1, true, true, FC[0], FC[1], IS, 4, k, islot, FP); } while (0)
#pragma unroll
    for (int n = 0; n < 12; ++n) G3_ISS(n & 3, n >> 2, n >> 2, f0);
    { unsigned sink = 0; touch(wo + (b * 4 + wid) * 2048, (lane & 15) * 128, sink); asm volatile("s_waitcnt vmcnt(0)" : "+v"(sink) :: "memory"); }
    await<GLD_ACQ>(ws, 1, 16, b); T_STAMP(3);
#pragma unroll
    for (int n = 4; n < 8; ++n) G3_ISS(n, 0, 0, f0);
#pragma unroll
    for (int n = 4; n < 8; ++n) G3_ISS(n, 1, 1, f1);
#pragma unroll
    for (int n = 4; n < 8; ++n) G3_ISS(n, 2, 2, f2);
    G3_STEP(8, 0, f0, f3, false, true, 3, 3);
    G3_STEP(12, 1, f1, f0, true, true, 4, 0);
    G3_STEP(16, 2, f2, f1, true, true, 5, 1);
    G3_STEP(16, 3, f3, f2, true, true, 6, 2);
    G3_STEP(16, 0, f0, f3, true, true, 7, 3);
    for (int k = 5; k < 29; k += 4) {
        G3_STEP(16, 1, f1, f0, true, true, k + 3, 0);
        G3_STEP(16, 2, f2, f1, true, true, k + 4, 1);
        G3_STEP(16, 3, f3, f2, true, true, k + 5, 2);
        G3_STEP(16, 0, f0, f3, true, true, k + 6, 3);
    }
    G3_STEP(16, 1, f1, f0, true, false, 0, 0);
    G3_STEP(8, 2, f2, f1, true, false, 0, 0);
    G3_STEP(0, 3, f3, f2, true, false, 0, 0);
    G3_HALF(0, 0, 0, false, true, f3[2], f3[3], false, 0, 0, 0, f0);
#undef G3_ISS
#undef G3_HALF
#undef G3_STEP
    MMA_DRAIN(); T_STAMP(4); T_STAMP(5);
    const int partner = b ^ 4;
#pragma unroll
    for (int u = 0; u < 4; ++u) st_wt(ws, (unsigned)WS_PX + partner * 16384 + (wid * 4 + u) * 1024 + lane * 16, p ? pack44(acc[0][u], acc[1][u]) : pack44(acc[0][4 + u], acc[1][4 + u]));
    publish(ws, 3, 1, partner, 0); T_STAMP(6);
    await<GLD_ACQ>(ws, 3, 1, b); T_STAMP(7);
    const char* gx = ws + WS_PX + b * 16384 + wid * 4096;
    gld1<0>(gx, vf, f0[0]); gld1<1024>(gx, vf, f0[1]); gld1<2048>(gx, vf, f0[2]); gld1<3072>(gx, vf, f0[3]);
    asm volatile("s_waitcnt vmcnt(0)" : "+v"(f0[0]), "+v"(f0[1]), "+v"(f0[2]), "+v"(f0[3]) :: "memory");
    const unsigned dst = (tb * 128 + p * 64 + rho) * 4096 + eb * 64 + g * 16;
#pragma unroll
    for (int u = 0; u < 4; ++u) {
        const u32x4 mine = p ? pack44(acc[0][4 + u], acc[1][4 + u]) : pack44(acc[0][u], acc[1][u]), other = __builtin_bit_cast(u32x4, f0[u]);
        const u32x4 re = p ? other : mine, im = p ? mine : other;
        st_out16(out, dst + u * 16 * 4096, (u32x4){sub_pk(re[0], im[0]), sub_pk(re[1], im[1]), sub_pk(re[2], im[2]), sub_pk(re[3], im[3])});
    }
}

__device__ __forceinline__ float xhalf_max(float m) { auto rr = __builtin_amdgcn_permlane32_swap(__float_as_uint(m), __float_as_uint(m), false, false);
    return __builtin_fmaxf(__uint_as_float(rr[0]), __uint_as_float(rr[1])); }
__device__ __forceinline__ float xhalf_sum(float m) { auto rr = __builtin_amdgcn_permlane32_swap(__float_as_uint(m), __float_as_uint(m), false, false);
    return __uint_as_float(rr[0]) + __uint_as_float(rr[1]); }
__device__ __forceinline__ float rowmax16(const f32x16& s) {
    float a = __builtin_fmaxf(s[0], s[1]);
#pragma unroll
    for (int r = 2; r < 16; r += 2) a = __builtin_fmaxf(__builtin_fmaxf(a, s[r]), s[r + 1]);
    return xhalf_max(a); }
__device__ __forceinline__ float max16(const f32x16& s, float a) {
#pragma unroll
    for (int r = 0; r < 16; r += 2) a = __builtin_fmaxf(__builtin_fmaxf(a, s[r]), s[r + 1]);
    return a; }
constexpr int REF = 64;
__device__ __forceinline__ unsigned rescale_pk(unsigned w, int d) {
    return cvtpk(__builtin_ldexpf(__uint_as_float(w << 16), d), __builtin_ldexpf(__uint_as_float(w & 0xffff0000u), d)); }

__device__ __forceinline__ void role_att(int b, char* ws, char* lds) {
    const int lane = threadIdx.x & 63, wid = __builtin_amdgcn_readfirstlane(threadIdx.x >> 6);
    const int x = b & 7, i = b >> 3;
    const int hd = x * 4 + (i >> 3), tt0 = (i & 7) * 8 + wid * 2;
    const char* gkv = ws + WS_KV + ((size_t)hd << 18);
    const unsigned vl = lane * 16;
    const unsigned ldw = (unsigned)(uintptr_t)lds + wid * 4096;
#define AT_DMA(st) dma4dev(gkv + (st) * 16384 + wid * 4096, ldw + ((st) & 3) * 16384, vl)
    await<GLD_ACQ>(ws, 0, 16, b); T_STAMP(3);
    AT_DMA(0); AT_DMA(1);
    const unsigned gq = (unsigned)WS_Q + ((hd * 64 + tt0) << 11) + vl;
    const bf16x8 q00 = ld_dev(ws, gq), q01 = ld_dev(ws, gq + 1024), q10 = ld_dev(ws, gq + 2048), q11 = ld_dev(ws, gq + 3072);
    bf16x8 sel0, sel1;
    { const bool on = ((lane >> 2) & 1) == ((lane >> 4) & 1); const short v0 = (on && (lane & 3) == 0) ? (short)0x3F80 : (short)0, v1 = (on && (lane & 3) == 1) ? (short)0x3F80 : (short)0;
      sel0 = (bf16x8){v0, v0, v0, v0, v0, v0, v0, v0}; sel1 = (bf16x8){v1, v1, v1, v1, v1, v1, v1, v1}; PIN(sel0); PIN(sel1); }
    const lds_cptr lp = (lds_cptr)lds + vl;
    f32x16 o0 = {}, o1 = {}, nm0, nm1, sA, sB = {}; f32x4 L = {};
    u32x4 pA0 = {}, pA1 = {}, pB0 = {}, pB1 = {};
    bf16x8 k0, k1, kn0, kn1, v0, v1, vp0 = {}, vp1 = {};
    WAIT_BAR(0);
    k0 = ldsr(lp); k1 = ldsr(lp + 1024);
    {
        const f32x16 z = {}; float m0 = -__builtin_inff(), m1 = m0;
#pragma unroll
        for (int kt = 0; kt < 4; ++kt) { const bf16x8 ka = ldsr(lp + kt * 4096), kb = ldsr(lp + kt * 4096 + 1024);
            m0 = max16(MFMA32(kb, q01, MFMA32(ka, q00, z)), m0); m1 = max16(MFMA32(kb, q11, MFMA32(ka, q10, z)), m1); }
        m0 = xhalf_max(m0) + (float)REF; m1 = xhalf_max(m1) + (float)REF;
#pragma unroll
        for (int r = 0; r < 16; ++r) { nm0[r] = -m0; nm1[r] = -m1; }
        PIN(nm0); PIN(nm1);
    }
    sA = MFMA32(k1, q01, MFMA32(k0, q00, nm0));
#define EX(x) __builtin_amdgcn_exp2f(x)
#define BF(x) __builtin_bit_cast(bf16x8, x)
#define E_(S, i) do { S[i] = EX(S[i]); SBAR(); } while (0)
#define C_(P, j, S, i) do { P[j] = cvtpk(S[i], S[i + 1]); SBAR(); } while (0)
#define BODY(SC, SN, PC0, PC1, PP0, PP1, KA, KB, QA, QB, NM, VA, VB, OP, SEL, LD0, LD1) do { \
        SN = MFMA32(KA, QA, NM); SBAR(); LD0; SBAR(); E_(SC, 0); E_(SC, 1); E_(SC, 2); E_(SC, 3); PIN(SC); SBAR(); \
        OP = MFMA32(VA, BF(PP0), OP); SBAR(); LD1; SBAR(); E_(SC, 4); E_(SC, 5); E_(SC, 6); E_(SC, 7); PIN(SC); SBAR(); \
        SN = MFMA32(KB, QB, SN); SBAR(); E_(SC, 8); E_(SC, 9); E_(SC, 10); E_(SC, 11); PIN(SC); SBAR(); \
        L = MFMA16(SEL, BF(PP0), L); SBAR(); E_(SC, 12); E_(SC, 13); PIN(SC); SBAR(); \
        L = MFMA16(SEL, BF(PP1), L); SBAR(); E_(SC, 14); E_(SC, 15); PIN(SC); SBAR(); \
        OP = MFMA32(VB, BF(PP1), OP); SBAR(); C_(PC0, 0, SC, 0); C_(PC0, 1, SC, 2); C_(PC0, 2, SC, 4); C_(PC0, 3, SC, 6); C_(PC1, 0, SC, 8); C_(PC1, 1, SC, 10); C_(PC1, 2, SC, 12); C_(PC1, 3, SC, 14); PIN(PC0); PIN(PC1); SBAR(); \
    } while (0)
#define KTILE(KO, KN) do { \
        BODY(sA, sB, pA0, pA1, pB0, pB1, k0, k1, q10, q11, nm1, vp0, vp1, o1, sel1, (kn0 = ldsr(lp + (KN)), kn1 = ldsr(lp + (KN) + 1024)), (v0 = ldsr(lp + (KO) + 2048), v1 = ldsr(lp + (KO) + 3072))); \
        BODY(sB, sA, pB0, pB1, pA0, pA1, kn0, kn1, q00, q01, nm0, v0, v1, o0, sel0, (void)0, (void)0); \
        k0 = kn0; k1 = kn1; vp0 = v0; vp1 = v1; } while (0)
    T_STAMP(4);
    for (int st = 0; st < 16; ++st) {
        if (st) WAIT_BAR(0);
        if (st + 2 < 16) AT_DMA(st + 2);
        if (__builtin_expect(st == 4 || __any(__builtin_fmaxf(L[0], L[1]) > 0x1p40f), 0)) {
            const int d0 = __builtin_amdgcn_frexp_expf(L[0]) + REF, d1 = __builtin_amdgcn_frexp_expf(L[1]) + REF;
            const float f0 = (float)d0, f1 = (float)d1;
            L[0] = __builtin_ldexpf(L[0], -d0); L[1] = __builtin_ldexpf(L[1], -d1);
#pragma unroll
            for (int r = 0; r < 16; ++r) { o0[r] = __builtin_ldexpf(o0[r], -d0); o1[r] = __builtin_ldexpf(o1[r], -d1); nm0[r] -= f0; nm1[r] -= f1; sA[r] -= f0; }
#pragma unroll
            for (int r = 0; r < 4; ++r) { pB0[r] = rescale_pk(pB0[r], -d1); pB1[r] = rescale_pk(pB1[r], -d1); }
        }
        const int so = (st & 3) * 16384, sn = ((st + 1) & 3) * 16384;
        KTILE(so, so + 4096); KTILE(so + 4096, so + 8192); KTILE(so + 8192, so + 12288); KTILE(so + 12288, sn);
    }
    o1 = MFMA32(vp0, BF(pB0), o1); L = MFMA16(sel1, BF(pB0), L); o1 = MFMA32(vp1, BF(pB1), o1); L = MFMA16(sel1, BF(pB1), L);
    T_STAMP(5);
    float l0 = L[0], l1 = L[1];
    {
        float t0 = 0.f, t1 = 0.f;
#pragma unroll
        for (int r = 0; r < 16; ++r) { t0 += __builtin_fabsf(o0[r]); t1 += __builtin_fabsf(o1[r]); }
        if (__builtin_expect(__any(!(t0 + t1 + l0 + l1 < __builtin_inff()) || !(l0 > 0.f) || !(l1 > 0.f) || !(t0 > 0x1p-100f) || !(t1 > 0x1p-100f)), 0)) {
            float m0 = -__builtin_inff(), m1 = m0; l0 = 0.f; l1 = 0.f; o0 = f32x16{}; o1 = f32x16{};
            for (int kt = 0; kt < 64; ++kt) {
                const unsigned g = (unsigned)WS_KV + (hd << 18) + kt * 4096 + vl;
                const bf16x8 ka = ld_dev(ws, g), kb = ld_dev(ws, g + 1024), va = ld_dev(ws, g + 2048), vb = ld_dev(ws, g + 3072);
                f32x16 z = {};
                f32x16 s0 = MFMA32(kb, q01, MFMA32(ka, q00, z)), s1 = MFMA32(kb, q11, MFMA32(ka, q10, z));
                const float n0 = __builtin_fmaxf(m0, rowmax16(s0)), n1 = __builtin_fmaxf(m1, rowmax16(s1));
                const float c0 = (n0 == -__builtin_inff()) ? 0.f : n0, c1 = (n1 == -__builtin_inff()) ? 0.f : n1;
                const float a0 = EX(m0 - c0), a1 = EX(m1 - c1); m0 = n0; m1 = n1;
#pragma unroll
                for (int r = 0; r < 16; ++r) { s0[r] = EX(s0[r] - c0); s1[r] = EX(s1[r] - c1); o0[r] *= a0; o1[r] *= a1; }
                const u32x4 a = pack8(s0, 0), bb = pack8(s0, 8), c = pack8(s1, 0), d = pack8(s1, 8);
                f32x4 ls = {}; ls = MFMA16(sel0, BF(a), ls); ls = MFMA16(sel0, BF(bb), ls); ls = MFMA16(sel1, BF(c), ls); ls = MFMA16(sel1, BF(d), ls);
                l0 = l0 * a0 + ls[0]; l1 = l1 * a1 + ls[1];
                o0 = MFMA32(vb, BF(bb), MFMA32(va, BF(a), o0)); o1 = MFMA32(vb, BF(d), MFMA32(va, BF(c), o1));
            }
        }
    }
    const float r0 = 1.0f / l0, r1 = 1.0f / l1;
    const unsigned go = (unsigned)WS_O + ((tt0 * 32 + hd) << 11) + ((lane >> 4) & 1) * 1024 + (lane >> 5) * 256 + (lane & 15) * 16;
    st_wt(ws, go, pack8(o0, 0, r0)); st_wt(ws, go + 512, pack8(o0, 8, r0));
    st_wt(ws, go + 65536, pack8(o1, 0, r1)); st_wt(ws, go + 65536 + 512, pack8(o1, 8, r1));
    publish(ws, 2, 32, g4_index((i & 7) * 2 + ((lane >> 4) & 1), lane & 15), hd);
#undef E_
#undef C_
#undef AT_DMA
#undef BODY
#undef KTILE
}

__device__ __forceinline__ void role_g4(int b, const char* wo, char* ws, char* out, char* lds) {
    const int lane = threadIdx.x & 63, wid = __builtin_amdgcn_readfirstlane(threadIdx.x >> 6);
    const int x = b & 7, i = b >> 3;
    const int tb = (x >> 1) * 4 + (i & 3), nb = (x & 1) * 8 + (i >> 2);
    const char* gw = wo + nb * 128;
    const char* gf = ws + WS_O + ((size_t)(tb * 4 + wid) << 16);
    unsigned vw[2];
#pragma unroll
    for (int j = 0; j < 2; ++j) vw[j] = nrow_off(wid * 16, j, lane, 2048);
    const unsigned vf = lane * 16;
    const unsigned ldw = (unsigned)(uintptr_t)lds + wid * 2048;
    const int rho = lane & 15, g = lane >> 4, q4 = (lane >> 2) & 3;
    const int k0 = 16 * (g >> 1) + 4 * (g & 1) + q4;
    const int ow = k0 * 128 + (tsw(k0) << 5) + (lane & 3) * 8;
    f32x4 acc[2][4] = {};
#pragma unroll
    for (int n = 0; n < 8; ++n) PIN(acc[n >> 2][n & 3]);
    bf16x8 fo[8][4]; s16x4 wl[2][4], wh[2][4];
    const int kr0 = 4 * (i & 3) + (x >> 1) + 2 * (i >> 2) + (x & 1);
#define G4_W(k) do { const char* w_ = gw + (((k) + kr0) & 15) * (64 * 2048); const unsigned d_ = ldw + ((k) % 10) * 8192; dma1<0>(w_, d_, vw[0]); dma1<1024>(w_, d_, vw[1]); } while (0)
#define G4_ISS(n, k) do { const char* f_ = gf + (((k) + kr0) & 15) * 4096; \
        if ((n) == 0) gld1<0>(f_, vf, fo[(k) & 7][0]); if ((n) == 1) gld1<1024>(f_, vf, fo[(k) & 7][1]); if ((n) == 2) gld1<2048>(f_, vf, fo[(k) & 7][2]); if ((n) == 3) gld1<3072>(f_, vf, fo[(k) & 7][3]); } while (0)
#define G4_HALF(RS, slot, KK, RD, MM, B0, B1, IS, k) do { \
        _Pragma("unroll") for (int n = 0; n < 4; ++n) { \
            if (RD) { const lds_cptr w_ = (lds_cptr)lds + (slot) * 8192 + (KK) * 4096; wl[RS][n] = ldstr(w_ + (ow ^ (n * 32))); wh[RS][n] = ldstr(w_ + (ow ^ (n * 32) ^ 64) + 1024); } \
            if (MM) { const bf16x8 w_ = cat(wl[RS ^ 1][n], wh[RS ^ 1][n]); mma16(acc[0][n], w_, B0); mma16(acc[1][n], w_, B1); } \
            if (IS) G4_ISS(n, k); \
            SBAR(); } } while (0)
#define G4_STEP(W, k) do { asm volatile("s_waitcnt vmcnt(%4)" : "+v"(fo[(k) & 7][0]), "+v"(fo[(k) & 7][1]), "+v"(fo[(k) & 7][2]), "+v"(fo[(k) & 7][3]) : "i"(W) : "memory"); \
        G4_HALF(0, (k) % 10, 0, true, (k) >= 1, fo[((k) + 7) & 7][2], fo[((k) + 7) & 7][3], false, 0); \
        G4_HALF(1, (k) % 10, 1, true, true, fo[(k) & 7][0], fo[(k) & 7][1], (k) >= 1 && (k) + 7 < 16, (k) + 7); } while (0)
#pragma unroll
    for (int k = 0; k < 10; ++k) G4_W(k);
    asm volatile("s_waitcnt vmcnt(0)" ::: "memory");
    await<GLD_ACQ>(ws, 2, 32, b); T_STAMP(7);
#pragma unroll
    for (int n = 0; n < 32; ++n) G4_ISS(n & 3, n >> 2);
    G4_STEP(28, 0); G4_STEP(24, 1); G4_STEP(24, 2); G4_STEP(24, 3); G4_STEP(24, 4); G4_STEP(24, 5);
    WAIT_BAR(63);
#pragma unroll
    for (int k = 10; k < 16; ++k) G4_W(k);
    G4_STEP(36, 6); G4_STEP(36, 7); G4_STEP(36, 8); G4_STEP(36, 9);
    WAIT_BAR(12);
    G4_STEP(12, 10); G4_STEP(12, 11); G4_STEP(12, 12); G4_STEP(8, 13); G4_STEP(4, 14); G4_STEP(0, 15);
    G4_HALF(0, 0, 0, false, true, fo[7][2], fo[7][3], false, 0);
#undef G4_W
#undef G4_ISS
#undef G4_HALF
#undef G4_STEP
    MMA_DRAIN();
    const unsigned d = (tb * 128 + wid * 32 + rho) * 4096 + 2048 + nb * 128 + g * 8;
#pragma unroll
    for (int u = 0; u < 2; ++u)
#pragma unroll
        for (int n = 0; n < 4; ++n) st_out8(out, d + u * 16 * 4096 + n * 32, (u32x2){cvtpk(acc[u][n][0], acc[u][n][1]), cvtpk(acc[u][n][2], acc[u][n][3])});
}

#define PRIO_B 3
__global__ __launch_bounds__(256, 2) void k_fused(const char* xA, const char* wsre, const char* wsim, const char* wre, const char* wim,
                                                   const char* xB, const char* wqkv, const char* wo, char* ws, char* out) {
    extern __shared__ __attribute__((aligned(16))) char lds[];
    T_STAMP(0);
    if (blockIdx.x < 256) {
        __builtin_amdgcn_s_setprio(PRIO_B);
        T_STAMP(9); role_g2(blockIdx.x, xB, wqkv, ws, lds); T_STAMP(2);
        role_att(blockIdx.x, ws, lds); T_STAMP(6);
        role_g4(blockIdx.x, wo, ws, out, lds); T_STAMP(8);
    } else {
        role_g1(blockIdx.x - 256, xA, wre, wim, ws, lds); T_STAMP(2);
        role_g3(blockIdx.x - 256, wsre, wsim, wo, ws, out, lds); T_STAMP(8);
    }
}

extern "C" void kernel_launch(void* const* d_in, const int* in_sizes, int n_in, void* d_out, int out_size, void* d_ws, size_t ws_size, hipStream_t stream) {
    (void)hipFuncSetAttribute((const void*)k_fused, hipFuncAttributeMaxDynamicSharedMemorySize, LDS_BYTES);
    k_fused<<<512, 256, LDS_BYTES, stream>>>((const char*)d_in[0], (const char*)d_in[1], (const char*)d_in[2], (const char*)d_in[3], (const char*)d_in[4],
                                             (const char*)d_in[5], (const char*)d_in[6], (const char*)d_in[7], (char*)d_ws, (char*)d_out);
}
```

```cpp
#include <hip/hip_runtime.h>
#include <hip/hip_bf16.h>

typedef short bf16x8 __attribute__((ext_vector_type(8)));
typedef short s16x4 __attribute__((ext_vector_type(4)));
typedef float f32x16 __attribute__((ext_vector_type(16)));
typedef float f32x4 __attribute__((ext_vector_type(4)));
typedef unsigned u32x4 __attribute__((ext_vector_type(4)));
#define LAS __attribute__((address_space(3)))
typedef LAS const char* lds_cptr;

#define MFMA32(a, b, c) __builtin_amdgcn_mfma_f32_32x32x16_bf16(a, b, c, 0, 0, 0)
#define MFMA16(a, b, c) __builtin_amdgcn_mfma_f32_16x16x32_bf16(a, b, c, 0, 0, 0)
#define SBAR() __builtin_amdgcn_sched_barrier(0)
#define PIN(x) asm volatile("" : "+v"(x))

constexpr int LDS_BYTES = 81920;
constexpr size_t WS_AB = 0;
constexpr size_t WS_KV = WS_AB + (8u << 20);
constexpr size_t WS_O = WS_KV + (8u << 20);
constexpr size_t WS_PX = WS_O + (4u << 20);
constexpr size_t WS_FLAG = WS_PX + (4u << 20);
constexpr unsigned long long MAGIC = 0x9E3779B97F4A7C15ull;
typedef __attribute__((address_space(1))) unsigned long long gu64;

typedef float f32x2 __attribute__((ext_vector_type(2)));
typedef __bf16 bf16x2_t __attribute__((ext_vector_type(2)));
__device__ __forceinline__ unsigned cvtpk(float lo, float hi) { return __builtin_bit_cast(unsigned, __builtin_convertvector((f32x2){lo, hi}, bf16x2_t)); }
__device__ __forceinline__ float single(float x) { asm("" : "+v"(x)); return x; }
__device__ __forceinline__ unsigned single(unsigned x) { asm("" : "+v"(x)); return x; }
__device__ __forceinline__ unsigned cvtpk(float lo, float hi, float sc) { return cvtpk(single(lo * sc), single(hi * sc)); }
__device__ __forceinline__ bool is_magic(unsigned long long v) { return (single((unsigned)v ^ (unsigned)MAGIC) | ((unsigned)(v >> 32) ^ (unsigned)(MAGIC >> 32))) == 0; }
template <typename V> __device__ __forceinline__ void zero(V& v) {
#pragma unroll
    for (int r = 0; r < (int)(sizeof(V) / 4); ++r) { float z; asm volatile("v_mov_b32 %0, 0" : "=v"(z)); v[r] = z; } }
__device__ __forceinline__ u32x4 pack8(const f32x16& a, int o, float sc) {
    return (u32x4){cvtpk(a[o], a[o + 1], sc), cvtpk(a[o + 2], a[o + 3], sc), cvtpk(a[o + 4], a[o + 5], sc), cvtpk(a[o + 6], a[o + 7], sc)}; }
__device__ __forceinline__ u32x4 pack8(const f32x16& a, int o) {
    return (u32x4){cvtpk(a[o], a[o + 1]), cvtpk(a[o + 2], a[o + 3]), cvtpk(a[o + 4], a[o + 5]), cvtpk(a[o + 6], a[o + 7])}; }
__device__ __forceinline__ bf16x8 ldsr(lds_cptr p) { return *(const LAS bf16x8*)p; }
typedef short v4i16_t __attribute__((ext_vector_type(4)));
__device__ __forceinline__ s16x4 ldstr(lds_cptr p) { return __builtin_bit_cast(s16x4, __builtin_amdgcn_ds_read_tr16_b64_v4i16((LAS v4i16_t*)p)); }
__device__ __forceinline__ bf16x8 cat(s16x4 a, s16x4 b) { return (bf16x8){a[0], a[1], a[2], a[3], b[0], b[1], b[2], b[3]}; }

__device__ __forceinline__ u32x4 pack44(const f32x4& a, const f32x4& b) { return (u32x4){cvtpk(a[0], a[1]), cvtpk(a[2], a[3]), cvtpk(b[0], b[1]), cvtpk(b[2], b[3])}; }
__device__ __forceinline__ u32x4 pack44(const f32x4& a, const f32x4& b, float s) { return (u32x4){cvtpk(a[0], a[1], s), cvtpk(a[2], a[3], s), cvtpk(b[0], b[1], s), cvtpk(b[2], b[3], s)}; }

template <int OFF> __device__ __forceinline__ void dma1(const char* sbase, unsigned lds, unsigned v) {
    asm volatile("s_mov_b32 m0, %1\n\ts_nop 0\n\tglobal_load_lds_dwordx4 %2, %0 offset:%3" :: "s"(sbase), "s"(lds), "v"(v), "i"(OFF) : "memory"); }
#define GLD_SC " sc1"
__device__ __forceinline__ void dma4dev(const char* sbase, unsigned lds, unsigned v) {
    asm volatile("s_mov_b32 m0, %1\n\ts_nop 0\n\t"
                 "global_load_lds_dwordx4 %2, %0" GLD_SC "\n\t"
                 "global_load_lds_dwordx4 %2, %0 offset:1024" GLD_SC "\n\t"
                 "global_load_lds_dwordx4 %2, %0 offset:2048" GLD_SC "\n\t"
                 "global_load_lds_dwordx4 %2, %0 offset:3072" GLD_SC
                 :: "s"(sbase), "s"(lds), "v"(v) : "memory"); }
__device__ __forceinline__ bf16x8 ld_dev(char* ws, unsigned off) {
    return __builtin_bit_cast(bf16x8, __builtin_amdgcn_raw_buffer_load_b128(__builtin_amdgcn_make_buffer_rsrc(ws, 0, 0x7fffffff, 0x00020000), off, 0, 16)); }
template <int OFF> __device__ __forceinline__ void gld1(const char* sbase, unsigned voff, bf16x8& a) {
    asm volatile("global_load_dwordx4 %0, %2, %1 offset:%3" GLD_SC : "=&v"(a) : "s"(sbase), "v"(voff), "i"(OFF) : "memory"); }
__device__ __forceinline__ void touch(const char* sbase, unsigned voff, unsigned& sink) {
    asm volatile("global_load_dword %0, %2, %1" : "+v"(sink) : "s"(sbase), "v"(voff) : "memory"); }
#define WAIT_BAR(N) do { __builtin_amdgcn_s_waitcnt(0xC07F); asm volatile("s_waitcnt vmcnt(" #N ")\n\ts_barrier" ::: "memory"); } while (0)
#define WAIT_BAR4(N, R) do { __builtin_amdgcn_s_waitcnt(0xC07F); asm volatile("s_waitcnt vmcnt(" #N ")\n\ts_barrier" : "+v"(R[0]), "+v"(R[1]), "+v"(R[2]), "+v"(R[3]) :: "memory"); } while (0)

__device__ __forceinline__ void mma16(f32x4& c, const bf16x8& a, const bf16x8& b) { asm volatile("v_mfma_f32_16x16x32_bf16 %0, %1, %2, %0" : "+v"(c) : "v"(a), "v"(b)); }
#define MMA_DRAIN() do { SBAR(); asm volatile("s_nop 7\n\ts_nop 7"); SBAR(); } while (0)
__device__ __forceinline__ void st_wt(char* ws, unsigned off, u32x4 v) {
    __builtin_amdgcn_raw_buffer_store_b128(v, __builtin_amdgcn_make_buffer_rsrc(ws, 0, 0x7fffffff, 0x00020000), off, 0, 16); }
__device__ __forceinline__ void st_out16(char* out, unsigned off, u32x4 v) {
    __builtin_amdgcn_raw_buffer_store_b128(v, __builtin_amdgcn_make_buffer_rsrc(out, 0, 8u << 20, 0x00020000), off, 0, 0);
}
typedef unsigned u32x2 __attribute__((ext_vector_type(2)));
__device__ __forceinline__ void st_out8(char* out, unsigned off, u32x2 v) {
    __builtin_amdgcn_raw_buffer_store_b64(v, __builtin_amdgcn_make_buffer_rsrc(out, 0, 8u << 20, 0x00020000), off, 0, 0);
}
__device__ __forceinline__ gu64* flag_at(char* ws, int kind, int consumer, int slot) { return (gu64*)(ws + WS_FLAG) + ((kind * 256 + consumer) * 32 + slot); }
__device__ __forceinline__ void publish(char* ws, int kind, int n, int consumer, int slot) {
    asm volatile("s_waitcnt vmcnt(0) lgkmcnt(0)\n\ts_barrier" ::: "memory");
    if (threadIdx.x < n) __hip_atomic_store(flag_at(ws, kind, consumer, slot), MAGIC, __ATOMIC_RELAXED, __HIP_MEMORY_SCOPE_AGENT);
}
#define POLL_NAP 4
__device__ __forceinline__ void await(char* ws, int kind, int n, int me) {
    if (__builtin_amdgcn_readfirstlane(threadIdx.x) == 0) {
        gu64* f = flag_at(ws, kind, me, (threadIdx.x & (n - 1)));
        const unsigned long long t0 = __builtin_amdgcn_s_memrealtime();
        while (!__all(is_magic(__hip_atomic_load(f, __ATOMIC_RELAXED, __HIP_MEMORY_SCOPE_AGENT)))) {
            if ((unsigned)(__builtin_amdgcn_s_memrealtime() - t0) > 20000000u) break;
            __builtin_amdgcn_s_sleep(POLL_NAP); }
        if (threadIdx.x < n) __hip_atomic_store(f, 0ull, __ATOMIC_RELAXED, __HIP_MEMORY_SCOPE_AGENT);
    }
    asm volatile("s_barrier" ::: "memory");
}

__device__ __forceinline__ void flag_peek(gu64* f, unsigned long long& v) { asm volatile("global_load_dwordx2 %0, %1, off sc1" : "=&v"(v) : "v"(f) : "memory"); }
template <int W> __device__ __forceinline__ void flag_join(gu64* f, int n, unsigned long long& v) {
    asm volatile("s_waitcnt vmcnt(%1)" : "+v"(v) : "i"(W) : "memory");
    if (__builtin_amdgcn_readfirstlane(threadIdx.x) == 0) {
        if (!__all(is_magic(v))) {
            const unsigned long long t0 = __builtin_amdgcn_s_memrealtime();
            while (!__all(is_magic(__hip_atomic_load(f, __ATOMIC_RELAXED, __HIP_MEMORY_SCOPE_AGENT)))) {
                if ((unsigned)(__builtin_amdgcn_s_memrealtime() - t0) > 20000000u) break;
                __builtin_amdgcn_s_sleep(POLL_NAP); }
        }
        if (threadIdx.x < n) __hip_atomic_store(f, 0ull, __ATOMIC_RELAXED, __HIP_MEMORY_SCOPE_AGENT);
    }
}

__device__ __forceinline__ int fsw(int r) { return ((r >> 1) & 7) ^ (((r >> 4) & 1) * 6); }
__device__ __forceinline__ int tsw(int k) { return ((k >> 1) & 1) | ((((k >> 2) ^ (k >> 3)) & 1) << 1); }
__device__ __forceinline__ int perm32(int rho, int T) { return 8 * (rho >> 2) + 4 * T + (rho & 3); }
__device__ __forceinline__ int tokmap(int rho, int T) { return 16 * (rho >> 3) + 8 * T + (rho & 7); }
__device__ __forceinline__ unsigned krow_off(int row0, int j, int lane, int ld) {
    const int r = row0 + 8 * j + (lane >> 3);
    return (unsigned)(r * ld + (((lane & 7) ^ fsw(r)) << 4) - 1024 * j); }
__device__ __forceinline__ unsigned nrow_off(int k0, int j, int lane, int ld) {
    const int k = k0 + 8 * j + (lane >> 3);
    return (unsigned)(k * ld + (((lane & 7) ^ (tsw(k) << 1)) << 4) - 1024 * j); }
__device__ __forceinline__ int frag_off(int row, int g) { return row * 128 + ((g ^ fsw(row)) << 4); }

#define RB 4
__device__ __forceinline__ int krot0(int x, int i) { return (i & 7) + (i >> 3); }
__device__ __forceinline__ int kidx(int k, int k0) { return (k & ~(RB - 1)) | ((k + k0) & (RB - 1)); }
__device__ __forceinline__ int g3_index(int p, int tb, int et) { return (p * 4 + (tb >> 2)) + 8 * ((tb & 3) + 4 * et); }
__device__ __forceinline__ int g2_index(int tm, int hp) { return ((tm >> 3) * 4 + (hp >> 2)) + 8 * ((tm & 7) + 8 * (hp & 3)); }
__device__ __forceinline__ int g4_index(int tb, int nb) { return ((tb >> 2) * 2 + (nb >> 3)) + 8 * ((tb & 3) + 4 * (nb & 7)); }

__device__ __forceinline__ void role_g1(int b, const char* xA, const char* wre, const char* wim, char* ws, char* lds) {
    const int lane = threadIdx.x & 63, wid = __builtin_amdgcn_readfirstlane(threadIdx.x >> 6);
    const int x = b & 7, i = b >> 3;
    const int sm = (x >> 2) * 8 + (i & 7), en = (x & 3) * 4 + (i >> 3);
    const int p = en >> 3, e0 = (en & 7) * 128, s0 = sm * 128;
    const char* ga = xA + (size_t)s0 * 2048;
    const char* gb = (p ? wim : wre) + (size_t)e0 * 2048;
    unsigned vo[4];
#pragma unroll
    for (int j = 0; j < 4; ++j) vo[j] = krow_off(wid * 32, j, lane, 2048);
    const unsigned ldw = (unsigned)(uintptr_t)lds + wid * 4096;
    const int wm = wid >> 1, wn = wid & 1, rho = lane & 15, g = lane >> 4;
    int oa[2], ob[2];
#pragma unroll
    for (int T = 0; T < 2; ++T) { oa[T] = frag_off(wm * 64 + perm32(rho, T), g); ob[T] = 16384 + frag_off(wn * 64 + perm32(rho, T), g); }
    f32x4 acc[4][4];
#pragma unroll
    for (int n = 0; n < 16; ++n) { zero(acc[n >> 2][n & 3]); PIN(acc[n >> 2][n & 3]); }
    bf16x8 fa[3][4], fb[3][4];
#define A_NAP 100
    __builtin_amdgcn_s_sleep(A_NAP / 3); __builtin_amdgcn_s_sleep(A_NAP / 3); __builtin_amdgcn_s_sleep(A_NAP / 3);

    const int k0 = krot0(x, i);
#define G1_DMA(n, kn, CUR) do { const unsigned d_ = ldw + (CUR); const int ko_ = kidx(kn, k0) * 128; \
        if ((n) == 0) dma1<0>(ga + ko_, d_, vo[0]); if ((n) == 1) dma1<0>(gb + ko_, d_ + 16384, vo[0]); \
        if ((n) == 2) dma1<1024>(ga + ko_, d_, vo[1]); if ((n) == 3) dma1<1024>(gb + ko_, d_ + 16384, vo[1]); \
        if ((n) == 4) dma1<2048>(ga + ko_, d_, vo[2]); if ((n) == 5) dma1<2048>(gb + ko_, d_ + 16384, vo[2]); \
        if ((n) == 6) dma1<3072>(ga + ko_, d_, vo[3]); if ((n) == 7) dma1<3072>(gb + ko_, d_ + 16384, vo[3]); } while (0)
#define G1_RD(RS, CUR, KK, n) do { if ((n) < 4) fa[RS][(n) & 3] = ldsr((lds_cptr)lds + (CUR) + (oa[(n) & 1] ^ ((KK) * 64)) + (((n) >> 1) & 1) * 4096); \
                                   else fb[RS][(n) & 3] = ldsr((lds_cptr)lds + (CUR) + (ob[(n) & 1] ^ ((KK) * 64)) + (((n) >> 1) & 1) * 4096); } while (0)
#define G1_MM(S, n) do { mma16(acc[(n) >> 1][((n) & 1) * 2], fa[S][(n) >> 1], fb[S][((n) & 1) * 2]); mma16(acc[(n) >> 1][((n) & 1) * 2 + 1], fa[S][(n) >> 1], fb[S][((n) & 1) * 2 + 1]); } while (0)
#define G1_STEP(W, CUR, SB, SC, MM, DM, kn) do { WAIT_BAR(W); \
        _Pragma("unroll") for (int n = 0; n < 8; ++n) { G1_RD(0, CUR, 0, n); G1_RD(SB, CUR, 1, n); if (MM) G1_MM(SC, n); SBAR(); } \
        if (DM) WAIT_BAR(63); \
        _Pragma("unroll") for (int n = 0; n < 8; ++n) { G1_MM(0, n); if (DM) G1_DMA(n, kn, CUR); SBAR(); } } while (0)
#pragma unroll
    for (int n = 0; n < 16; ++n) G1_DMA(n & 7, n >> 3, (n >> 3) * 32768);
    G1_STEP(8, 0, 1, 2, false, true, 2);
    for (int kt = 1; kt < 13; kt += 2) { G1_STEP(8, 32768, 2, 1, true, true, kt + 2); G1_STEP(8, 0, 1, 2, true, true, kt + 3); }
    G1_STEP(8, 32768, 2, 1, true, true, 15); G1_STEP(8, 0, 1, 2, true, false, 0); G1_STEP(0, 32768, 2, 1, true, false, 0);
#pragma unroll
    for (int n = 0; n < 8; ++n) G1_MM(2, n);
#undef G1_DMA
#undef G1_MM
#undef G1_STEP
#undef G1_RD
    MMA_DRAIN();
#pragma unroll
    for (int blk = 0; blk < 2; ++blk)
#pragma unroll
        for (int n = 0; n < 4; ++n) {
            const int eb = (e0 >> 5) + wn * 2 + (n >> 1), sb = (s0 >> 5) + wm * 2 + blk;
            st_wt(ws, (unsigned)WS_AB + ((((p * 32 + eb) * 64 + sb) * 2 + (n & 1)) << 10) + lane * 16, pack44(acc[2 * blk][n], acc[2 * blk + 1][n]));
        }
    publish(ws, 1, 16, g3_index(p, lane & 15, en & 7), sm);
}

__device__ __forceinline__ void role_g2(int b, const char* xB, const char* wqkv, char* ws, char* lds) {
    const int lane = threadIdx.x & 63, wid = __builtin_amdgcn_readfirstlane(threadIdx.x >> 6);
    const int x = b & 7, i = b >> 3;
    const int tm = (x >> 2) * 8 + (i & 7), hp = (x & 3) * 4 + (i >> 3);
    const char* ga = xB + (size_t)tm * 128 * 2048;
    const char* gw = wqkv + hp * 128;
    unsigned vo[4], vw[2];
#pragma unroll
    for (int j = 0; j < 4; ++j) vo[j] = krow_off(wid * 32, j, lane, 2048);
#pragma unroll
    for (int j = 0; j < 2; ++j) vw[j] = nrow_off(wid * 16, j, lane, 6144);
    const unsigned lds0 = (unsigned)(uintptr_t)lds;
    const int wm = wid >> 1, wn = wid & 1, rho = lane & 15, g = lane >> 4, q4 = (lane >> 2) & 3;
    int oa[2];
#pragma unroll
    for (int T = 0; T < 2; ++T) oa[T] = frag_off(wm * 64 + tokmap(rho, T), g);
    const int ow = 16384 + (8 * g + q4) * 128 + (((2 * wn) ^ tsw(8 * g + q4)) << 5) + (lane & 3) * 8;
    f32x4 acc[4][6] = {};
#pragma unroll
    for (int n = 0; n < 24; ++n) PIN(acc[n / 6][n % 6]);
    bf16x8 fa[3][4]; s16x4 wl[3][6], wh[3][6];
    const int k0 = krot0(x, i);
#define G2_DMA(n, kt, buf) do { const unsigned d_ = lds0 + (buf); const int kr_ = kidx(kt, k0); const char* w_ = gw + kr_ * (64 * 6144); const char* a_ = ga + kr_ * 128; \
        if ((n) == 0) dma1<0>(a_, d_ + wid * 4096, vo[0]); if ((n) == 1) dma1<0>(w_, d_ + 16384 + wid * 2048, vw[0]); if ((n) == 2) dma1<1024>(w_, d_ + 16384 + wid * 2048, vw[1]); \
        if ((n) == 3) dma1<1024>(a_, d_ + wid * 4096, vo[1]); if ((n) == 4) dma1<0>(w_ + 2048, d_ + 24576 + wid * 2048, vw[0]); if ((n) == 5) dma1<1024>(w_ + 2048, d_ + 24576 + wid * 2048, vw[1]); \
        if ((n) == 6) dma1<2048>(a_, d_ + wid * 4096, vo[2]); if ((n) == 7) dma1<0>(w_ + 4096, d_ + 32768 + wid * 2048, vw[0]); if ((n) == 8) dma1<1024>(w_ + 4096, d_ + 32768 + wid * 2048, vw[1]); \
        if ((n) == 9) dma1<3072>(a_, d_ + wid * 4096, vo[3]); } while (0)
#define G2_RDA(RS, CUR, KK, m) fa[RS][m] = ldsr((lds_cptr)lds + (CUR) + (oa[(m) & 1] ^ ((KK) * 64)) + ((m) >> 1) * 4096)
#define G2_RDW(RS, CUR, KK, c) do { const lds_cptr w_ = (lds_cptr)lds + (CUR) + ((c) >> 1) * 8192 + (KK) * 4096; \
        wl[RS][c] = ldstr(w_ + (ow ^ (((c) & 1) * 32))); wh[RS][c] = ldstr(w_ + (ow ^ (((c) & 1) * 32) ^ 64) + 512); } while (0)
#define G2_MM(S, m, c) do { const bf16x8 w_ = cat(wl[S][c], wh[S][c]); if ((c) < 4) mma16(acc[m][c], w_, fa[S][m]); else mma16(acc[m][c], fa[S][m], w_); } while (0)
#define G2_STEP(W, CUR, SB, SC, MM, DM, kn) do { WAIT_BAR(W); \
        _Pragma("unroll") for (int n = 0; n < 12; ++n) { \
            if (n < 6) G2_RDW(0, CUR, 0, (n < 6 ? n : 0)); else G2_RDW(SB, CUR, 1, (n < 6 ? 0 : n - 6)); \
            if (n < 4) G2_RDA(0, CUR, 0, (n & 3)); else if (n < 8) G2_RDA(SB, CUR, 1, (n & 3)); \
            if (MM) { G2_MM(SC, (n & 1) * 2, n >> 1); G2_MM(SC, (n & 1) * 2 + 1, n >> 1); } \
            SBAR(); } \
        if (DM) WAIT_BAR(63); \
        _Pragma("unroll") for (int n = 0; n < 12; ++n) { G2_MM(0, (n & 1) * 2, n >> 1); G2_MM(0, (n & 1) * 2 + 1, n >> 1); if (DM) G2_DMA(n, kn, CUR); SBAR(); } } while (0)
#pragma unroll
    for (int n = 0; n < 20; ++n) G2_DMA(n % 10, n / 10, (n / 10) * 40960);
    G2_STEP(10, 0, 1, 2, false, true, 2);
    for (int kt = 1; kt < 13; kt += 2) { G2_STEP(10, 40960, 2, 1, true, true, kt + 2); G2_STEP(10, 0, 1, 2, true, true, kt + 3); }
    G2_STEP(10, 40960, 2, 1, true, true, 15); G2_STEP(10, 0, 1, 2, true, false, 0); G2_STEP(0, 40960, 2, 1, true, false, 0);
#pragma unroll
    for (int n = 0; n < 12; ++n) { G2_MM(2, (n & 1) * 2, n >> 1); G2_MM(2, (n & 1) * 2 + 1, n >> 1); }
#undef G2_DMA
#undef G2_RDA
#undef G2_RDW
#undef G2_MM
#undef G2_STEP
    MMA_DRAIN();
    constexpr float QS = 0.17677669529663687f * 1.4426950408889634f;
    const int co = (g >> 1) * 1024 + (g & 1) * 512;
    WAIT_BAR(63);
#pragma unroll
    for (int blk = 0; blk < 2; ++blk) {
        const int lo = wm * 16384 + wn * 8192 + blk * 4096 + co;
        const unsigned kv = (unsigned)WS_KV + ((hp * 16 + tm) << 15) + lo;
#pragma unroll
        for (int T = 0; T < 2; ++T) {
            const int to = tokmap(rho, T) * 16, vo_ = 2048 + T * 256 + rho * 16;
            const u32x4 kf = pack44(acc[2 * blk + T][2], acc[2 * blk + T][3]), vf = pack44(acc[2 * blk][4 + T], acc[2 * blk + 1][4 + T]);
            st_wt(ws, kv + to, kf); st_wt(ws, kv + vo_, vf);
            *(LAS u32x4*)((LAS char*)lds + lo + to) = kf; *(LAS u32x4*)((LAS char*)lds + lo + vo_) = vf;
            *(LAS u32x4*)((LAS char*)lds + 32768 + wid * 4096 + blk * 2048 + co + to) = pack44(acc[2 * blk + T][0], acc[2 * blk + T][1], QS);
        }
    }
}

__device__ __forceinline__ unsigned sub_pk(unsigned a, unsigned b) {
    return cvtpk(single(__uint_as_float(a << 16) - __uint_as_float(b << 16)), single(__uint_as_float(a & 0xffff0000u) - __uint_as_float(b & 0xffff0000u))); }
__device__ __forceinline__ void role_g3(int b, const char* wsre, const char* wsim, const char* wo, char* ws, char* out, char* lds) {
    const int lane = threadIdx.x & 63, wid = __builtin_amdgcn_readfirstlane(threadIdx.x >> 6);
    const int x = b & 7, i = b >> 3;
    const int p = x >> 2, tb = (x & 3) * 4 + (i & 3), eb = (i >> 2) * 4 + wid;
    const char* gt = (p ? wsim : wsre) + (size_t)tb * 128 * 4096;
    const char* gf = ws + WS_AB + ((size_t)(p * 32 + eb) << 17);
    unsigned vo[4];
#pragma unroll
    for (int j = 0; j < 4; ++j) vo[j] = krow_off((wid ^ (2 * p)) * 32, j, lane, 4096);
    const unsigned vf = lane * 16;
    const unsigned ldw = (unsigned)(uintptr_t)lds + wid * 4096;
    const int rho = lane & 15, g = lane >> 4;
    int ob[2];
#pragma unroll
    for (int T = 0; T < 2; ++T) ob[T] = frag_off(16 * T + rho, g);
    f32x4 acc[2][8];
#pragma unroll
    for (int n = 0; n < 16; ++n) { zero(acc[n >> 3][n & 7]); PIN(acc[n >> 3][n & 7]); }
    bf16x8 f0[4], f1[4], f2[4], f3[4], ft[2][8];
    const int kr0 = (i >> 2) + 2 * (i & 3);
#define G3_ISS(n, k, slot, F) do { const int kr_ = ((k) & ~7) | (((k) + kr0) & 7); const char* t_ = gt + kr_ * 128; const char* f_ = gf + kr_ * 4096; const unsigned d_ = ldw + (slot) * 16384; \
        if ((n) == 0) dma1<0>(t_, d_, vo[0]); if ((n) == 1) dma1<1024>(t_, d_, vo[1]); if ((n) == 2) dma1<2048>(t_, d_, vo[2]); if ((n) == 3) dma1<3072>(t_, d_, vo[3]); \
        if ((n) == 4) gld1<0>(f_, vf, F[0]); if ((n) == 5) gld1<1024>(f_, vf, F[1]); if ((n) == 6) gld1<2048>(f_, vf, F[2]); if ((n) == 7) gld1<3072>(f_, vf, F[3]); } while (0)
#define G3_HALF(RS, slot, KK, RD, MM, A0, A1, IS, I0, k, islot, F) do { \
        _Pragma("unroll") for (int n = 0; n < 8; ++n) { \
            if (RD) ft[RS][n] = ldsr((lds_cptr)lds + (slot) * 16384 + (ob[n & 1] ^ ((KK) * 64)) + (n >> 1) * 4096); \
            if (MM) { mma16(acc[0][n], A0, ft[RS ^ 1][n]); mma16(acc[1][n], A1, ft[RS ^ 1][n]); } \
            if ((IS) && (n & 1) == 0) G3_ISS((I0) + (n >> 1), k, islot, F); \
            SBAR(); } } while (0)
#define G3_STEP(W, slot, FC, FP, MM, IS, k, islot) do { WAIT_BAR4(W, FC); \
        G3_HALF(0, slot, 0, true, MM, FP[2], FP[3], IS, 0, k, islot, FP); G3_HALF(1, slot, 1, true, true, FC[0], FC[1], IS, 4, k, islot, FP); } while (0)
    unsigned sink = 0;
#pragma unroll
    for (int n = 0; n < 12; ++n) G3_ISS(n & 3, n >> 2, n >> 2, f0);
    { touch(wo + (b * 4 + wid) * 2048, (lane & 15) * 128, sink); asm volatile("s_waitcnt vmcnt(0)" : "+v"(sink) :: "memory"); }
    await(ws, 1, 16, b);

#pragma unroll
    for (int n = 4; n < 8; ++n) G3_ISS(n, 0, 0, f0);
#pragma unroll
    for (int n = 4; n < 8; ++n) G3_ISS(n, 1, 1, f1);
#pragma unroll
    for (int n = 4; n < 8; ++n) G3_ISS(n, 2, 2, f2);
    G3_STEP(8, 0, f0, f3, false, true, 3, 3);
    PIN(sink);
    G3_STEP(12, 1, f1, f0, true, true, 4, 0);
    G3_STEP(16, 2, f2, f1, true, true, 5, 1);
    G3_STEP(16, 3, f3, f2, true, true, 6, 2);
    G3_STEP(16, 0, f0, f3, true, true, 7, 3);
    for (int k = 5; k < 29; k += 4) {
        G3_STEP(16, 1, f1, f0, true, true, k + 3, 0);
        G3_STEP(16, 2, f2, f1, true, true, k + 4, 1);
        G3_STEP(16, 3, f3, f2, true, true, k + 5, 2);
        G3_STEP(16, 0, f0, f3, true, true, k + 6, 3);
    }
    G3_STEP(16, 1, f1, f0, true, false, 0, 0);
    G3_STEP(8, 2, f2, f1, true, false, 0, 0);
    G3_STEP(0, 3, f3, f2, true, false, 0, 0);
    G3_HALF(0, 0, 0, false, true, f3[2], f3[3], false, 0, 0, 0, f0);
#undef G3_ISS
#undef G3_HALF
#undef G3_STEP
    MMA_DRAIN();
    const int partner = b ^ 4;
#pragma unroll
    for (int u = 0; u < 4; ++u) st_wt(ws, (unsigned)WS_PX + partner * 16384 + (wid * 4 + u) * 1024 + lane * 16, pack44(acc[0][4 + u], acc[1][4 + u]));
    publish(ws, 3, 1, partner, 0);
    await(ws, 3, 1, b);
    const char* gx = ws + WS_PX + b * 16384 + wid * 4096;
    gld1<0>(gx, vf, f0[0]); gld1<1024>(gx, vf, f0[1]); gld1<2048>(gx, vf, f0[2]); gld1<3072>(gx, vf, f0[3]);
    asm volatile("s_waitcnt vmcnt(0)" : "+v"(f0[0]), "+v"(f0[1]), "+v"(f0[2]), "+v"(f0[3]) :: "memory");
    const unsigned dst = (tb * 128 + p * 64 + rho) * 4096 + eb * 64 + g * 16, sg = p ? 0x80008000u : 0u;
#pragma unroll
    for (int u = 0; u < 4; ++u) {
        const u32x4 mine = pack44(acc[0][u], acc[1][u]), other = __builtin_bit_cast(u32x4, f0[u]);
        st_out16(out, dst + u * 16 * 4096, (u32x4){sub_pk(mine[0] ^ sg, other[0] ^ sg), sub_pk(mine[1] ^ sg, other[1] ^ sg), sub_pk(mine[2] ^ sg, other[2] ^ sg), sub_pk(mine[3] ^ sg, other[3] ^ sg)});
    }
}

__device__ __forceinline__ float xhalf_max(float m) { auto rr = __builtin_amdgcn_permlane32_swap(__float_as_uint(m), __float_as_uint(m), false, false);
    return __builtin_fmaxf(__uint_as_float(rr[0]), __uint_as_float(rr[1])); }
__device__ __forceinline__ float rowmax16(const f32x16& s) {
    float a = __builtin_fmaxf(s[0], s[1]);
#pragma unroll
    for (int r = 2; r < 16; r += 2) a = __builtin_fmaxf(__builtin_fmaxf(a, s[r]), s[r + 1]);
    return xhalf_max(a); }
__device__ __forceinline__ float max16(const f32x16& s, float a) {
#pragma unroll
    for (int r = 0; r < 16; r += 2) a = __builtin_fmaxf(__builtin_fmaxf(a, s[r]), s[r + 1]);
    return a; }
constexpr int REF = 64;
__device__ __forceinline__ unsigned rescale_pk(unsigned w, int d) {
    return cvtpk(__builtin_ldexpf(__uint_as_float(w << 16), d), __builtin_ldexpf(__uint_as_float(w & 0xffff0000u), d)); }

__device__ __forceinline__ void wait_bar8() { asm volatile("s_waitcnt vmcnt(8)\n\ts_barrier" ::: "memory"); }
__device__ __forceinline__ void role_att(int b, char* ws, char* lds) {
    const int lane = threadIdx.x & 63, wid = __builtin_amdgcn_readfirstlane(threadIdx.x >> 6);
    const int x = b & 7, i = b >> 3;
    const int tm = (x >> 2) * 8 + (i & 7), hp = (x & 3) * 4 + (i >> 3);
    const int hd = hp * 2 + (wid & 1), tt0 = tm * 4 + (wid >> 1) * 2;
    const char* gkv = ws + WS_KV + ((size_t)hp << 19) + wid * 4096;
    const unsigned vl = lane * 16;
    const unsigned ldw = (unsigned)(uintptr_t)lds + wid * 4096;
#define AT_SRC(u) (gkv + (((((tm ^ ((u) >> 1)) & 8) | ((tm + ((u) >> 1)) & 7)) * 2 + ((u) & 1)) << 14))
#define AT_DMA(u, so) dma4dev(AT_SRC(u), ldw + (so), vl)
    WAIT_BAR(63);
    const lds_cptr lq = (lds_cptr)lds + 32768 + wid * 4096 + vl;
    const bf16x8 q00 = ldsr(lq), q01 = ldsr(lq + 1024), q10 = ldsr(lq + 2048), q11 = ldsr(lq + 3072);
    bf16x8 sel0, sel1;
    { const bool on = ((lane >> 2) & 1) == ((lane >> 4) & 1); const short v0 = (on && (lane & 3) == 0) ? (short)0x3F80 : (short)0, v1 = (on && (lane & 3) == 1) ? (short)0x3F80 : (short)0;
      sel0 = (bf16x8){v0, v0, v0, v0, v0, v0, v0, v0}; sel1 = (bf16x8){v1, v1, v1, v1, v1, v1, v1, v1}; PIN(sel0); PIN(sel1); }
    const lds_cptr lp = (lds_cptr)lds + vl + (wid & 1) * 8192;
    f32x16 o0, o1, nm0, nm1, sA, sB; f32x4 L = {};
    zero(o0); zero(o1);
    u32x4 pA0 = {}, pA1 = {}, pB0 = {}, pB1 = {};
    bf16x8 k0, k1, kn0, kn1, v0, v1, vp0 = {}, vp1 = {};
    k0 = ldsr(lp); k1 = ldsr(lp + 1024);
    {
        const f32x16 z = {}; float m0 = -__builtin_inff(), m1 = m0;
#pragma unroll
        for (int kt = 0; kt < 4; ++kt) { const bf16x8 ka = ldsr(lp + (kt >> 1) * 16384 + (kt & 1) * 4096), kb = ldsr(lp + (kt >> 1) * 16384 + (kt & 1) * 4096 + 1024);
            m0 = max16(MFMA32(kb, q01, MFMA32(ka, q00, z)), m0); m1 = max16(MFMA32(kb, q11, MFMA32(ka, q10, z)), m1); }
        m0 = xhalf_max(m0) + (float)REF; m1 = xhalf_max(m1) + (float)REF;
#pragma unroll
        for (int r = 0; r < 16; ++r) { nm0[r] = -m0; nm1[r] = -m1; }
        PIN(nm0); PIN(nm1);
    }
    sA = MFMA32(k1, q01, MFMA32(k0, q00, nm0));
#define EX(x) __builtin_amdgcn_exp2f(x)
#define BF(x) __builtin_bit_cast(bf16x8, x)
#define E_(S, i) do { S[i] = EX(S[i]); SBAR(); } while (0)
#define C_(P, j, S, i) do { P[j] = cvtpk(S[i], S[i + 1]); SBAR(); } while (0)
#define BODY(SC, SN, PC0, PC1, PP0, PP1, KA, KB, QA, QB, NM, VA, VB, OP, SEL, LD0, LD1) do { \
        SN = MFMA32(KA, QA, NM); SBAR(); LD0; SBAR(); E_(SC, 0); E_(SC, 1); E_(SC, 2); E_(SC, 3); PIN(SC); SBAR(); \
        OP = MFMA32(VA, BF(PP0), OP); SBAR(); LD1; SBAR(); E_(SC, 4); E_(SC, 5); E_(SC, 6); E_(SC, 7); PIN(SC); SBAR(); \
        SN = MFMA32(KB, QB, SN); SBAR(); E_(SC, 8); E_(SC, 9); E_(SC, 10); E_(SC, 11); PIN(SC); SBAR(); \
        L = MFMA16(SEL, BF(PP0), L); SBAR(); E_(SC, 12); E_(SC, 13); PIN(SC); SBAR(); \
        L = MFMA16(SEL, BF(PP1), L); SBAR(); E_(SC, 14); E_(SC, 15); PIN(SC); SBAR(); \
        OP = MFMA32(VB, BF(PP1), OP); SBAR(); C_(PC0, 0, SC, 0); C_(PC0, 1, SC, 2); C_(PC0, 2, SC, 4); C_(PC0, 3, SC, 6); C_(PC1, 0, SC, 8); C_(PC1, 1, SC, 10); C_(PC1, 2, SC, 12); C_(PC1, 3, SC, 14); PIN(PC0); PIN(PC1); SBAR(); \
    } while (0)
#define KTILE(KO, KN, X0, X1) do { \
        BODY(sA, sB, pA0, pA1, pB0, pB1, k0, k1, q10, q11, nm1, vp0, vp1, o1, sel1, (kn0 = ldsr(KN), kn1 = ldsr((KN) + 1024)), (v0 = ldsr((KO) + 2048), v1 = ldsr((KO) + 3072))); \
        BODY(sB, sA, pB0, pB1, pA0, pA1, kn0, kn1, q00, q01, nm0, v0, v1, o0, sel0, X0, X1); \
        k0 = kn0; k1 = kn1; vp0 = v0; vp1 = v1; } while (0)

    KTILE(lp, lp + 4096, (void)0, (void)0);
    publish(ws, 0, 16, g2_index(lane & 15, hp), ((tm ^ lane) & 8) | ((tm - lane) & 7));
    KTILE(lp + 4096, lp + 16384, (void)0, (void)0); KTILE(lp + 16384, lp + 20480, (void)0, (void)0);
    await(ws, 0, 8, b);
    AT_DMA(2, 32768); AT_DMA(3, 49152); AT_DMA(4, 65536); AT_DMA(5, 0); WAIT_BAR(12);
    gu64* const fl = flag_at(ws, 0, b, 8 + (lane & 7)); unsigned long long fv;
    flag_peek(fl, fv);
    KTILE(lp + 20480, lp + 32768, (void)0, (void)0);
    bool big = false; int s0 = 32768;
    for (int u = 2; u < 32; u += 2) {
        if (__builtin_expect(big, 0)) {
            const int d0 = __builtin_amdgcn_frexp_expf(L[0]) + REF, d1 = __builtin_amdgcn_frexp_expf(L[1]) + REF;
            const float f0 = (float)d0, f1 = (float)d1;
            L[0] = __builtin_ldexpf(L[0], -d0); L[1] = __builtin_ldexpf(L[1], -d1);
#pragma unroll
            for (int r = 0; r < 16; ++r) { o0[r] = __builtin_ldexpf(o0[r], -d0); o1[r] = __builtin_ldexpf(o1[r], -d1); nm0[r] -= f0; nm1[r] -= f1; sA[r] -= f0; }
#pragma unroll
            for (int r = 0; r < 4; ++r) { pB0[r] = rescale_pk(pB0[r], -d1); pB1[r] = rescale_pk(pB1[r], -d1); }
            if (u == 8) flag_join<63>(fl, 8, fv);
        }
        const lds_cptr lc = lp + s0; lds_cptr l1, l2; const char* ga; unsigned da; int s1, s2;
#define NXT(s) ((s) == 65536 ? 0 : (s) + 16384)
        KTILE(lc, lc + 4096, (ga = AT_SRC(u + 4), da = ldw + (s0 == 0 ? 65536 : s0 - 16384), s1 = NXT(s0), l1 = lp + s1, wait_bar8()), dma4dev(ga, da, vl));
        KTILE(lc + 4096, l1, (void)0, (void)0);
        KTILE(l1, l1 + 4096, (s2 = NXT(s1), l2 = lp + s2, wait_bar8()), dma4dev(ga + 16384, ldw + s0, vl));
        KTILE(l1 + 4096, l2, (void)0, (big = (__builtin_amdgcn_ballot_w64(L[0] + L[1] > 0x1p40f) != 0) | (u == 6)));
        s0 = s2;
    }
    o1 = MFMA32(vp0, BF(pB0), o1); L = MFMA16(sel1, BF(pB0), L); o1 = MFMA32(vp1, BF(pB1), o1); L = MFMA16(sel1, BF(pB1), L);

    float l0 = L[0], l1 = L[1];
    {
        float t0 = 0.f, t1 = 0.f;
#pragma unroll
        for (int r = 0; r < 16; ++r) { t0 = single(t0 + __builtin_fabsf(o0[r])); t1 = single(t1 + __builtin_fabsf(o1[r])); }
        if (__builtin_expect(__any(!(t0 + t1 + l0 + l1 < __builtin_inff()) || !(l0 > 0.f) || !(l1 > 0.f) || !(t0 > 0x1p-100f) || !(t1 > 0x1p-100f)), 0)) {
            float m0 = -__builtin_inff(), m1 = m0; l0 = 0.f; l1 = 0.f; o0 = f32x16{}; o1 = f32x16{};
            for (int kt = 0; kt < 64; ++kt) {
                const unsigned g = (unsigned)WS_KV + (hp << 19) + (kt >> 1) * 16384 + (wid & 1) * 8192 + (kt & 1) * 4096 + vl;
                const bf16x8 ka = ld_dev(ws, g), kb = ld_dev(ws, g + 1024), va = ld_dev(ws, g + 2048), vb = ld_dev(ws, g + 3072);
                f32x16 z = {};
                f32x16 s0 = MFMA32(kb, q01, MFMA32(ka, q00, z)), s1 = MFMA32(kb, q11, MFMA32(ka, q10, z));
                const float n0 = __builtin_fmaxf(m0, rowmax16(s0)), n1 = __builtin_fmaxf(m1, rowmax16(s1));
                const float c0 = (n0 == -__builtin_inff()) ? 0.f : n0, c1 = (n1 == -__builtin_inff()) ? 0.f : n1;
                const float a0 = EX(m0 - c0), a1 = EX(m1 - c1); m0 = n0; m1 = n1;
#pragma unroll
                for (int r = 0; r < 16; ++r) { s0[r] = EX(s0[r] - c0); s1[r] = EX(s1[r] - c1); o0[r] *= a0; o1[r] *= a1; }
                const u32x4 a = pack8(s0, 0), bb = pack8(s0, 8), c = pack8(s1, 0), d = pack8(s1, 8);
                f32x4 ls = {}; ls = MFMA16(sel0, BF(a), ls); ls = MFMA16(sel0, BF(bb), ls); ls = MFMA16(sel1, BF(c), ls); ls = MFMA16(sel1, BF(d), ls);
                l0 = l0 * a0 + ls[0]; l1 = l1 * a1 + ls[1];
                o0 = MFMA32(vb, BF(bb), MFMA32(va, BF(a), o0)); o1 = MFMA32(vb, BF(d), MFMA32(va, BF(c), o1));
            }
        }
    }
    const float r0 = 1.0f / l0, r1 = 1.0f / l1;
    const unsigned go = (unsigned)WS_O + ((tt0 * 32 + hd) << 11) + ((lane >> 4) & 1) * 1024 + (lane >> 5) * 256 + (lane & 15) * 16;
    st_wt(ws, go, pack8(o0, 0, r0)); st_wt(ws, go + 512, pack8(o0, 8, r0));
    st_wt(ws, go + 65536, pack8(o1, 0, r1)); st_wt(ws, go + 65536 + 512, pack8(o1, 8, r1));
    publish(ws, 2, 16, g4_index(tm, lane & 15), hp);
#undef E_
#undef C_
#undef AT_DMA
#undef BODY
#undef KTILE
}

__device__ __forceinline__ void role_g4(int b, const char* wo, char* ws, char* out, char* lds) {
    const int lane = threadIdx.x & 63, wid = __builtin_amdgcn_readfirstlane(threadIdx.x >> 6);
    const int x = b & 7, i = b >> 3;
    const int tb = __builtin_amdgcn_readfirstlane((x >> 1) * 4 + (i & 3)), nb = (x & 1) * 8 + (i >> 2);
    const char* gw = wo + nb * 128;
    const char* gf = ws + WS_O + ((size_t)(tb * 4 + wid) << 16);
    unsigned vw[2];
#pragma unroll
    for (int j = 0; j < 2; ++j) vw[j] = nrow_off(wid * 16, j, lane, 2048);
    const unsigned vf = lane * 16;
    const unsigned ldw = (unsigned)(uintptr_t)lds + wid * 2048;
    const int rho = lane & 15, g = lane >> 4, q4 = (lane >> 2) & 3;
    const int k0 = 16 * (g >> 1) + 4 * (g & 1) + q4;
    const int ow = k0 * 128 + (tsw(k0) << 5) + (lane & 3) * 8;
    f32x4 acc[2][4];
#pragma unroll
    for (int n = 0; n < 8; ++n) { zero(acc[n >> 2][n & 3]); PIN(acc[n >> 2][n & 3]); }
    bf16x8 fo[8][4]; s16x4 wl[2][4], wh[2][4];
    const int kr0 = __builtin_amdgcn_readfirstlane(4 * (i & 3) + (x >> 1) + 2 * (i >> 2) + (x & 1));
#define G4_W(k) do { const char* w_ = gw + (((k) + kr0) & 15) * (64 * 2048); const unsigned d_ = ldw + ((k) % 10) * 8192; dma1<0>(w_, d_, vw[0]); dma1<1024>(w_, d_, vw[1]); } while (0)
#define G4_ISS(n, k) do { const char* f_ = gf + (((k) + kr0) & 15) * 4096; \
        if ((n) == 0) gld1<0>(f_, vf, fo[(k) & 7][0]); if ((n) == 1) gld1<1024>(f_, vf, fo[(k) & 7][1]); if ((n) == 2) gld1<2048>(f_, vf, fo[(k) & 7][2]); if ((n) == 3) gld1<3072>(f_, vf, fo[(k) & 7][3]); } while (0)
#define G4_HALF(RS, slot, KK, RD, MM, B0, B1, IS, k) do { \
        _Pragma("unroll") for (int n = 0; n < 4; ++n) { \
            if (RD) { const lds_cptr w_ = (lds_cptr)lds + (slot) * 8192 + (KK) * 4096; wl[RS][n] = ldstr(w_ + (ow ^ (n * 32))); wh[RS][n] = ldstr(w_ + (ow ^ (n * 32) ^ 64) + 1024); } \
            if (MM) { const bf16x8 w_ = cat(wl[RS ^ 1][n], wh[RS ^ 1][n]); mma16(acc[0][n], w_, B0); mma16(acc[1][n], w_, B1); } \
            if (IS) G4_ISS(n, k); \
            SBAR(); } } while (0)
#define G4_STEP(W, k) do { asm volatile("s_waitcnt vmcnt(%4)" : "+v"(fo[(k) & 7][0]), "+v"(fo[(k) & 7][1]), "+v"(fo[(k) & 7][2]), "+v"(fo[(k) & 7][3]) : "i"(W) : "memory"); \
        G4_HALF(0, (k) % 10, 0, true, (k) >= 1, fo[((k) + 7) & 7][2], fo[((k) + 7) & 7][3], false, 0); \
        G4_HALF(1, (k) % 10, 1, true, true, fo[(k) & 7][0], fo[(k) & 7][1], (k) >= 1 && (k) + 7 < 16, (k) + 7); } while (0)
#pragma unroll
    for (int k = 0; k < 10; ++k) G4_W(k);
    asm volatile("s_waitcnt vmcnt(0)" ::: "memory");
    await(ws, 2, 16, b);
#pragma unroll
    for (int n = 0; n < 32; ++n) G4_ISS(n & 3, n >> 2);
    G4_STEP(28, 0); G4_STEP(24, 1); G4_STEP(24, 2); G4_STEP(24, 3); G4_STEP(24, 4); G4_STEP(24, 5);
    WAIT_BAR(63);
#pragma unroll
    for (int k = 10; k < 16; ++k) G4_W(k);
    G4_STEP(36, 6); G4_STEP(36, 7); G4_STEP(36, 8); G4_STEP(36, 9);
    WAIT_BAR(12);
    G4_STEP(12, 10); G4_STEP(12, 11); G4_STEP(12, 12); G4_STEP(8, 13); G4_STEP(4, 14); G4_STEP(0, 15);
    G4_HALF(0, 0, 0, false, true, fo[7][2], fo[7][3], false, 0);
#undef G4_W
#undef G4_ISS
#undef G4_HALF
#undef G4_STEP
    MMA_DRAIN();
    const unsigned d = (tb * 128 + wid * 32 + rho) * 4096 + 2048 + nb * 128 + g * 8;
#pragma unroll
    for (int u = 0; u < 2; ++u)
#pragma unroll
        for (int n = 0; n < 4; ++n) st_out8(out, d + u * 16 * 4096 + n * 32, (u32x2){cvtpk(acc[u][n][0], acc[u][n][1]), cvtpk(acc[u][n][2], acc[u][n][3])});
}

#define PRIO_B 3
__global__ __launch_bounds__(256, 2) void k_fused(const char* xA, const char* wsre, const char* wsim, const char* wre, const char* wim,
                                                   const char* xB, const char* wqkv, const char* wo, char* ws, char* out) {
    extern __shared__ __attribute__((aligned(16))) char lds[];

    if (blockIdx.x < 256) {
        __builtin_amdgcn_s_setprio(PRIO_B);
        role_g2(blockIdx.x, xB, wqkv, ws, lds);
        role_att(blockIdx.x, ws, lds);
        role_g4(blockIdx.x, wo, ws, out, lds);
    } else {
        role_g1(blockIdx.x - 256, xA, wre, wim, ws, lds);
        role_g3(blockIdx.x - 256, wsre, wsim, wo, ws, out, lds);
    }
}

extern "C" void kernel_launch(void* const* d_in, const int* in_sizes, int n_in, void* d_out, int out_size, void* d_ws, size_t ws_size, hipStream_t stream) {
    (void)hipFuncSetAttribute((const void*)k_fused, hipFuncAttributeMaxDynamicSharedMemorySize, LDS_BYTES);
    k_fused<<<512, 256, LDS_BYTES, stream>>>((const char*)d_in[0], (const char*)d_in[1], (const char*)d_in[2], (const char*)d_in[3], (const char*)d_in[4],
                                             (const char*)d_in[5], (const char*)d_in[6], (const char*)d_in[7], (char*)d_ws, (char*)d_out);
}
```
